```python
import torch

def _softmax(x, axis=-1):
    return torch.softmax(x, dim=axis)
def _gelu(x):
    return torch.nn.functional.gelu(x, approximate="tanh")
def _silu(x):
    return torch.nn.functional.silu(x)
def _sigmoid(x):
    return torch.sigmoid(x)
def _layernorm(x, axis=-1, eps=1e-5):
    m = x.mean(dim=axis, keepdim=True)
    v = x.var(dim=axis, keepdim=True, unbiased=False)
    return (x - m) / torch.sqrt(v + eps)
def _scatter_add(tgt, idx, src, axis=0):
    return tgt.clone().index_add_(axis, idx, src)
def _segment_sum(src, seg, n):
    out = torch.zeros((n,) + src.shape[1:], dtype=src.dtype)
    return out.index_add_(0, seg, src)
def _pad(x, pw):
    flat = tuple(v for pair in reversed(pw) for v in pair)
    return torch.nn.functional.pad(x, flat)

kernel_name = 'gated_mix__ssm_conv_h3__dense_activation_silu__prompt'

def setup_inputs():

    _w = (lambda a: (lambda t: t.to(torch.bfloat16) if t.is_floating_point() else t)(torch.as_tensor(a)))
    def _A_setup():
        B, S, D = 1, 2048, 512
        ks = 4
        return {
            "q": _w((lambda *s: torch.randn(*s, dtype=torch.bfloat16) * 0.05)(B, S, D)),
            "k": _w((lambda *s: torch.randn(*s, dtype=torch.bfloat16) * 0.2)(B, S, D)),
            "v": _w((lambda *s: torch.randn(*s, dtype=torch.bfloat16) * 0.2)(B, S, D)),
            "shift_k": _w((lambda *s: torch.randn(*s, dtype=torch.bfloat16) * 0.2)(D, ks) * 0.5),
            "ssm_k": _w((lambda *s: torch.randn(*s, dtype=torch.bfloat16) * 0.4)(D, S) * (1.0 / S ** 0.5)),
            "ssm_D": _w((lambda *s: torch.randn(*s, dtype=torch.bfloat16) * 0.2)(D)),
        }
    _A_in = _A_setup()

    def _B_setup():
        B, S, D, F = 1, 2048, 512, 1792
        return {
            "x": _w((lambda *s: torch.randn(*s, dtype=torch.bfloat16) * 0.05)(B, S, D)),
            "w1": _w((lambda *s: torch.randn(*s, dtype=torch.bfloat16) * 3.2)(D, F) * (D ** -0.5)),
            "w2": _w((lambda *s: torch.randn(*s, dtype=torch.bfloat16) * 0.05)(F, D) * (F ** -0.5)),
        }
    _B_in = _B_setup()

    return {
        "A_q": _A_in["q"],
        "A_k": _A_in["k"],
        "A_v": _A_in["v"],
        "A_shift_k": _A_in["shift_k"],
        "A_ssm_k": _A_in["ssm_k"],
        "A_ssm_D": _A_in["ssm_D"],
        "B_x": _B_in["x"],
        "B_w1": _B_in["w1"],
        "B_w2": _B_in["w2"],
        "gate_w": _w((lambda *s: torch.randn(*s, dtype=torch.bfloat16) * 0.05)(512) * 16.0),
    }

def reference(A_q, A_k, A_v, A_shift_k, A_ssm_k, A_ssm_D, B_x, B_w1, B_w2, gate_w):
    def _A_ref(q, k, v, shift_k, ssm_k, ssm_D):
        B, S, D = q.shape
        ks = shift_k.shape[-1]
        k_pad = _pad(k, ((0, 0), (ks - 1, 0), (0, 0)))
        k_shift = torch.zeros_like(k)
        for t in range(ks):
            k_shift = k_shift + k_pad[:, t:t + S, :] * shift_k[:, ks - 1 - t][None, None, :]
        kv = k_shift * v
        L = 2 * S
        kv_pad = _pad(kv, ((0, 0), (0, S), (0, 0)))
        kf_pad = _pad(ssm_k, ((0, 0), (0, S)))
        kv_f = (lambda x, axes=(-2,-1): torch.fft.rfft2(x.float(), dim=axes))(kv_pad[:, None, :, :], axes=(1, 2))
        kf_f = (lambda x, axes=(-2,-1): torch.fft.rfft2(x.float(), dim=axes))(kf_pad[:, None, :], axes=(1, 2))
        kf_f = (lambda x, *d: x.permute(*d))(kf_f, 1, 2, 0)
        y_f = kv_f * kf_f[None, :, :, :]
        y = (lambda x, s, axes=(-2,-1): torch.fft.irfft2(x, s=s, dim=axes).to(torch.bfloat16))(y_f, (1, L), axes=(1, 2))[:, 0, :S, :]
        y = y + kv * ssm_D[None, None, :]
        return q * y

    def _B_ref(x, w1, w2):
        h = torch.einsum("bsd,df->bsf", x, w1)
        h = _silu(h)
        out = torch.einsum("bsf,fd->bsd", h, w2)
        return out

    x = A_q
    g = _sigmoid((x * gate_w).sum(-1, keepdims=True))
    a = _A_ref(x, A_k, A_v, A_shift_k, A_ssm_k, A_ssm_D)
    b = _B_ref(x, B_w1, B_w2)
    return g * a + (1 - g) * b
```

```cpp
#include <hip/hip_runtime.h>
#include <stdint.h>

typedef unsigned v4u __attribute__((ext_vector_type(4)));
typedef unsigned v2u __attribute__((ext_vector_type(2)));
typedef float f32x4 __attribute__((ext_vector_type(4)));
typedef float f32x2 __attribute__((ext_vector_type(2)));
typedef short bf16x8 __attribute__((ext_vector_type(8)));
typedef short s16x4 __attribute__((ext_vector_type(4)));
typedef __bf16 bf2 __attribute__((ext_vector_type(2)));
typedef unsigned long long u64;
#define LAS __attribute__((address_space(3)))

#define KSTAMP 0

constexpr unsigned WS_EPOCH = 0;
constexpr unsigned WS_F0 = 4096;
constexpr unsigned WS_FH = 8192;
constexpr unsigned WS_FY = 12288;
constexpr unsigned WS_GP = 16384;
constexpr unsigned WS_TS = 262144;
constexpr unsigned WS_KVT = 1u << 20;
constexpr unsigned WS_Y2 = 3u << 20;
constexpr unsigned WS_H = 5u << 20;

constexpr unsigned L_T = 131072;
constexpr unsigned L_FILT = 0;
constexpr unsigned L_U = 33280;
constexpr unsigned L_RED = 43008;
constexpr unsigned LDS_BYTES = 139264;

constexpr int SPIN_CAP = 1 << 16;

__device__ __forceinline__ unsigned pk_bf16(float a, float b) {
  f32x2 v = {a, b};
  return __builtin_bit_cast(unsigned, __builtin_convertvector(v, bf2));
}
__device__ __forceinline__ float blo(unsigned u) { return __builtin_bit_cast(float, u << 16); }
__device__ __forceinline__ float bhi(unsigned u) { return __builtin_bit_cast(float, u & 0xffff0000u); }
__device__ __forceinline__ f32x4 mfma16(v4u a, v4u b, f32x4 c) {
  return __builtin_amdgcn_mfma_f32_16x16x32_bf16(__builtin_bit_cast(bf16x8, a), __builtin_bit_cast(bf16x8, b), c, 0, 0, 0);
}
__device__ __forceinline__ float sigmoidf_(float x) {
  return __builtin_amdgcn_rcpf(1.0f + __builtin_amdgcn_exp2f(-1.4426950408889634f * x));
}

#define GLD1_SC1(dst, voff, sbase, imm) asm volatile("global_load_dword %0, %1, %2 offset:%3 sc1" : "=v"(dst) : "v"(voff), "s"(sbase), "i"(imm))
#define GLD2(dst, voff, sbase, imm) asm volatile("global_load_dwordx2 %0, %1, %2 offset:%3" : "=v"(dst) : "v"(voff), "s"(sbase), "i"(imm))
#define GLD2_SC1(dst, voff, sbase, imm) asm volatile("global_load_dwordx2 %0, %1, %2 offset:%3 sc1" : "=v"(dst) : "v"(voff), "s"(sbase), "i"(imm))
#define GLD4(dst, voff, sbase, imm) asm volatile("global_load_dwordx4 %0, %1, %2 offset:%3" : "=v"(dst) : "v"(voff), "s"(sbase), "i"(imm))
#define GLD4_SC1(dst, voff, sbase, imm) asm volatile("global_load_dwordx4 %0, %1, %2 offset:%3 sc1" : "=v"(dst) : "v"(voff), "s"(sbase), "i"(imm))
#define GST4_SC1(voff, data, sbase, imm) asm volatile("global_store_dwordx4 %0, %1, %2 offset:%3 sc1\n\ts_nop 1" ::"v"(voff), "v"(data), "s"(sbase), "i"(imm) : "memory")
#define GST4(voff, data, sbase, imm) asm volatile("global_store_dwordx4 %0, %1, %2 offset:%3\n\ts_nop 1" ::"v"(voff), "v"(data), "s"(sbase), "i"(imm) : "memory")
#define GST2_SC1(voff, data, sbase, imm) asm volatile("global_store_dwordx2 %0, %1, %2 offset:%3 sc1 ; FLAG" ::"v"(voff), "v"(data), "s"(sbase), "i"(imm) : "memory")
#define GST1_SC1(voff, data, sbase, imm) asm volatile("global_store_dword %0, %1, %2 offset:%3 sc1" ::"v"(voff), "v"(data), "s"(sbase), "i"(imm) : "memory")
#define DMA16(ldsaddr, voff, sbase) asm volatile("s_mov_b32 m0, %0\n\ts_nop 0\n\tglobal_load_lds_dwordx4 %1, %2" ::"s"(ldsaddr), "v"(voff), "s"(sbase) : "memory")
#define WAITV(n) asm volatile("s_waitcnt vmcnt(%0)" ::"i"(n) : "memory")
#define WAITV_BAR(n) asm volatile("s_waitcnt vmcnt(%0) lgkmcnt(0)\n\ts_barrier" ::"i"(n) : "memory")
#define BAR() asm volatile("s_waitcnt lgkmcnt(0)\n\ts_barrier" ::: "memory")
#define TIE(x) asm volatile("" : "+v"(x))
#define SCHED() __builtin_amdgcn_sched_barrier(0)
#define FRESH_IDS()                                   \
  int tid = threadIdx.x;                              \
  asm volatile("" : "+v"(tid));                       \
  const int lane = tid & 63, li = lane & 15, g = lane >> 4; \
  (void)li; (void)g;

#define STAMP(i) do {} while (0)

__device__ __forceinline__ bool all_eq(v2u f, unsigned tlo, unsigned thi) {
  return __builtin_amdgcn_ballot_w64(f.x == tlo && f.y == thi) == ~0ull;
}
__device__ __forceinline__ void spin_flags(v2u f, unsigned voff, const char* ws, unsigned tlo, unsigned thi) {
  int spins = 0;
  while (!all_eq(f, tlo, thi)) {
    if (++spins > SPIN_CAP) break;
    __builtin_amdgcn_s_sleep(2);
    asm volatile("global_load_dwordx2 %0, %1, %2 sc1 ; SPIN\n\ts_waitcnt vmcnt(0) ; SPIN" : "=&v"(f) : "v"(voff), "s"(ws) : "memory");
  }
}

#define W2A 8
#define W2B 8
#define W2C 8

template <bool g1>
__device__ __forceinline__ void body(const char* __restrict__ q, const char* __restrict__ k, const char* __restrict__ v,
                                     const char* __restrict__ sk, const char* __restrict__ ssmk, const char* __restrict__ ssmD,
                                     const char* __restrict__ w1, const char* __restrict__ w2, const char* __restrict__ gw,
                                     char* out, char* ws) {
  extern __shared__ __attribute__((aligned(16))) char smem_[];
  LAS char* const L = (LAS char*)smem_;
  const unsigned lbase = (unsigned)(uintptr_t)L;
  STAMP(0);
  const int tid = threadIdx.x, lane = tid & 63;
  const int w = __builtin_amdgcn_readfirstlane(tid >> 6);
  const int id = blockIdx.x, xcd = id & 7, loc = id >> 3;
  const int st = 4 * xcd + (loc >> 3), n = loc & 7;
  const int R = 4 * (xcd >> 1) + (g1 ? loc / 7 : 0), c = 7 * (xcd & 1) + loc % 7;
  const int li = lane & 15, g = lane >> 4;
  const int dpair = 32 * n + st;

  const unsigned vx0 = (unsigned)((16 * w + (lane >> 3)) * 1024 + (((lane & 7) ^ ((lane >> 4) & 7)) * 16));
  const unsigned vx1 = (unsigned)((16 * w + 8 + (lane >> 3)) * 1024 + (((lane & 7) ^ ((4 + (lane >> 4)) & 7)) * 16));
  const unsigned vw0 = (unsigned)((8 * w + g) * 3584 + (((li >> 1) ^ (g | ((w & 1) << 2))) * 32) + (li & 1) * 16);
  const char* const xb = q + (size_t)R * 131072;
  const char* const wb = w1 + c * 256;
#define ISSUE_CHUNK(ch)                                                               \
  do {                                                                                \
    if (g1) {                                                                         \
      const unsigned sl_ = lbase + ((ch) & 3) * 32768 + w * 2048;                     \
      DMA16(sl_, vx0, xb + (ch) * 128);                                               \
      DMA16(sl_ + 1024, vx1, xb + (ch) * 128);                                        \
      DMA16(sl_ + 16384, vw0, wb + (ch) * 229376);                                    \
      DMA16(sl_ + 16384 + 1024, vw0, wb + (ch) * 229376 + 14336);                     \
    }                                                                                 \
  } while (0)
#define CHUNK_WAIT(n)                   \
  do {                                  \
    if (g1) { WAITV_BAR(n); }           \
    else { WAITV_BAR(0); }              \
  } while (0)

  ISSUE_CHUNK(0);
  ISSUE_CHUNK(1);

  u64 epoch;
  unsigned dd;
  asm volatile("s_load_dwordx2 %0, %1, 0x0" : "=s"(epoch) : "s"(ws));
  asm volatile("s_load_dword %0, %1, 0x0" : "=s"(dd) : "s"(ssmD + dpair * 4));

  v2u kr[5], vr[2];
  v4u skr[2], qr, gwr;
  const int r0 = 64 * st + 8 * w + 2 * g;
  {
    const unsigned colo = (unsigned)(n * 128 + li * 8);
#pragma unroll
    for (int i = 0; i < 5; ++i) {
      int rr = r0 - 3 + i;
      rr = rr < 0 ? 0 : rr;
      const unsigned vo = (unsigned)rr * 1024u + colo;
      GLD2(kr[i], vo, k, 0);
    }
    const unsigned vo = (unsigned)r0 * 1024u + colo;
    GLD2(vr[0], vo, v, 0);
    GLD2(vr[1], vo, v, 1024);
    const unsigned so = (unsigned)(n * 512 + li * 32);
    GLD4(skr[0], so, sk, 0);
    GLD4(skr[1], so, sk, 16);
  }
  const unsigned qoff = (unsigned)((64 * st + 16 * (w >> 1) + li) * 1024 + n * 128 + g * 32 + (w & 1) * 16);
  GLD4(qr, qoff, q, 0);
  {
    const unsigned go = (unsigned)(n * 128 + g * 32 + (w & 1) * 16);
    GLD4(gwr, go, gw, 0);
  }
  ISSUE_CHUNK(2);
  ISSUE_CHUNK(3);

  f32x4 acc1[2][4];
#pragma unroll
  for (int a = 0; a < 2; ++a)
#pragma unroll
    for (int b = 0; b < 4; ++b) acc1[a][b] = f32x4{0.f, 0.f, 0.f, 0.f};
  const int wsx = w & 1, wf = w >> 1;
  const unsigned ax0 = (unsigned)((wsx * 64 + li) * 128 + ((g ^ (li >> 1)) * 16));
  const unsigned sg = (unsigned)((li >> 2) | ((g & 1) << 2));
  const unsigned aw0 = (unsigned)(16384 + (8 * g + (li >> 2)) * 256 + (((2 * wf) ^ sg) * 32) + (lane & 3) * 8);
#define COMPUTE_CHUNK(ch)                                                                                          \
  do {                                                                                                             \
    if (g1) {                                                                                                      \
      LAS char* const sl_ = L + ((ch) & 3) * 32768;                                                                \
      v4u xf[2][4], wfr[2][2];                                                                                     \
      _Pragma("unroll") for (int ks = 0; ks < 2; ++ks) {                                                           \
        _Pragma("unroll") for (int t2 = 0; t2 < 2; ++t2) {                                                         \
          const s16x4 a_ = __builtin_amdgcn_ds_read_tr16_b64_v4i16((LAS s16x4*)(sl_ + (aw0 ^ (t2 * 32)) + ks * 8192));        \
          const s16x4 b_ = __builtin_amdgcn_ds_read_tr16_b64_v4i16((LAS s16x4*)(sl_ + (aw0 ^ (t2 * 32)) + ks * 8192 + 1024)); \
          const v2u a2 = __builtin_bit_cast(v2u, a_), b2 = __builtin_bit_cast(v2u, b_);                            \
          wfr[ks][t2] = v4u{a2.x, a2.y, b2.x, b2.y};                                                               \
        }                                                                                                          \
        _Pragma("unroll") for (int i = 0; i < 4; ++i) xf[ks][i] = *(LAS v4u*)(sl_ + (ax0 ^ (ks * 64)) + i * 2048); \
        SCHED();                                                                                                   \
      }                                                                                                            \
      _Pragma("unroll") for (int ks = 0; ks < 2; ++ks)                                                             \
        _Pragma("unroll") for (int i = 0; i < 4; ++i)                                                              \
          _Pragma("unroll") for (int t2 = 0; t2 < 2; ++t2) acc1[t2][i] = mfma16(wfr[ks][t2], xf[ks][i], acc1[t2][i]); \
      SCHED();                                                                                                     \
    }                                                                                                              \
  } while (0)

  v2u w2r[56];
  unsigned vw2;
  const char* const w2b = w2 + (size_t)w * 229376 + n * 128;
#define ISSUE_W2(lo, hi)                                                                         \
  do {                                                                                           \
    _Pragma("unroll") for (int m_ = (lo); m_ < (hi); ++m_) {                                     \
      GLD2(w2r[m_], vw2, w2b + (m_ >> 3) * 32768 + ((m_ >> 2) & 1) * 16384, (m_ & 3) * 1024);    \
    }                                                                                            \
  } while (0)

  CHUNK_WAIT(23);
  asm volatile("" : "+s"(epoch), "+s"(dd));
  const u64 tag = epoch * 6364136223846793005ull + 1442695040888963407ull;
  const unsigned tlo = (unsigned)tag, thi = (unsigned)(tag >> 32);
  const v2u tagv = v2u{tlo, thi};
  STAMP(1);
  COMPUTE_CHUNK(0);
  CHUNK_WAIT(19);
  ISSUE_CHUNK(4);
  COMPUTE_CHUNK(1);
  CHUNK_WAIT(8);
  ISSUE_CHUNK(5);
  {
    FRESH_IDS();
#pragma unroll
    for (int i = 0; i < 5; ++i) TIE(kr[i]);
    TIE(vr[0]); TIE(vr[1]); TIE(skr[0]); TIE(skr[1]); TIE(qr); TIE(gwr);
    const int r0 = 64 * st + 8 * w + 2 * g;
#pragma unroll
    for (int i = 0; i < 3; ++i)
      if (r0 - 3 + i < 0) kr[i] = v2u{0u, 0u};
#pragma unroll
    for (int i = 0; i < 4; ++i) {
      float kk[5];
#pragma unroll
      for (int r = 0; r < 5; ++r) { const unsigned u = (i < 2) ? kr[r].x : kr[r].y; kk[r] = (i & 1) ? bhi(u) : blo(u); }
      const unsigned s0 = skr[i >> 1][(i & 1) * 2], s1 = skr[i >> 1][(i & 1) * 2 + 1];
      const float t0 = blo(s0), t1 = bhi(s0), t2 = blo(s1), t3 = bhi(s1);
      const unsigned u0 = (i < 2) ? vr[0].x : vr[0].y, u1 = (i < 2) ? vr[1].x : vr[1].y;
      const float v0 = (i & 1) ? bhi(u0) : blo(u0), v1 = (i & 1) ? bhi(u1) : blo(u1);
      const float a0 = kk[3] * t0 + kk[2] * t1 + kk[1] * t2 + kk[0] * t3;
      const float a1 = kk[4] * t0 + kk[3] * t1 + kk[2] * t2 + kk[1] * t3;
      *(LAS unsigned*)(L + L_T + (4 * li + i) * 128 + ((w ^ (li & 7)) * 16) + g * 4) = pk_bf16(a0 * v0, a1 * v1);
    }
    float gp = 0.f;
#pragma unroll
    for (int i = 0; i < 4; ++i) gp += blo(qr[i]) * blo(gwr[i]) + bhi(qr[i]) * bhi(gwr[i]);
    gp += __shfl_xor(gp, 16);
    gp += __shfl_xor(gp, 32);
    const unsigned gpo = (unsigned)(WS_GP + ((4 * st + (w >> 1)) * 16 + 2 * n + (w & 1)) * 64 + li * 4);
    GST1_SC1(gpo, gp, ws, 0);
  }
  COMPUTE_CHUNK(2);
  CHUNK_WAIT(9);
  {
    FRESH_IDS();
    const int d = 8 * w + (lane >> 3);
    v4u t = *(LAS v4u*)(L + L_T + d * 128 + (((lane & 7) ^ ((d >> 2) & 7)) * 16));
    const unsigned ko = (unsigned)(WS_KVT + (64 * n + d) * 4096 + st * 128 + (lane & 7) * 16);
    GST4_SC1(ko, t, ws, 0);
  }
  ISSUE_CHUNK(6);
  COMPUTE_CHUNK(3);
  CHUNK_WAIT(10);
  ISSUE_CHUNK(7);
  v4u fhi;
  v2u flo;
  {
    FRESH_IDS();
    const int ch = tid >> 8, bi = tid & 255;
    const unsigned fo = (unsigned)((2 * dpair + ch) * 4096 + (255 - bi) * 16);
    GLD4(fhi, fo, ssmk, 0);
    const unsigned fo2 = bi == 255 ? fo : fo - 8;
    GLD2(flo, fo2, ssmk, 0);
  }
  COMPUTE_CHUNK(4);
  CHUNK_WAIT(10);
  if (threadIdx.x == 0) {
    const unsigned fo = (unsigned)(WS_F0 + (n * 32 + st) * 8);
    GST2_SC1(fo, tagv, ws, 0);
  }
  STAMP(2);
  { FRESH_IDS(); vw2 = (unsigned)(g * 4096 + li * 8); }
  ISSUE_W2(0, W2A);
  COMPUTE_CHUNK(5);
  CHUNK_WAIT(6 + W2A);
  v2u pf;
  unsigned pf0;
  { FRESH_IDS(); pf0 = (unsigned)(WS_F0 + (n * 32 + 8 * (w & 3) + (lane & 7)) * 8); }
  GLD2_SC1(pf, pf0, ws, 0);
  ISSUE_W2(W2A, W2A + W2B);
  COMPUTE_CHUNK(6);
  CHUNK_WAIT(W2B);
  TIE(pf);
  if (!g1 && id == 8 * 28 && w == 0) {
#pragma unroll 1
    for (int part = 0; part < 4; ++part) {
      const unsigned eo = (unsigned)(WS_F0 + (part * 64 + lane) * 8);
      spin_flags(v2u{~tlo, 0u}, eo, ws, tlo, thi);
    }
    if (lane == 0) { const unsigned z = 0; GST2_SC1(z, tagv, ws, 0); }
  }
  spin_flags(pf, pf0, ws, tlo, thi);
  v4u kvt;
  {
    FRESH_IDS();
    const unsigned ko = (unsigned)(WS_KVT + (2 * dpair + (w >> 2)) * 4096 + (w & 3) * 1024 + lane * 16);
    GLD4_SC1(kvt, ko, ws, 0);
  }
  ISSUE_W2(W2A + W2B, W2A + W2B + W2C);
  COMPUTE_CHUNK(7);
  STAMP(3);

  if (g1) {
    FRESH_IDS();
    const unsigned ho = (unsigned)(WS_H + ((8 * R + 4 * wsx) * 56 + 4 * c + wf) * 1024 + lane * 16);
#pragma unroll
    for (int i = 0; i < 4; ++i) {
      float s_[8];
#pragma unroll
      for (int t2 = 0; t2 < 2; ++t2)
#pragma unroll
        for (int r = 0; r < 4; ++r) { const float x_ = acc1[t2][i][r]; s_[t2 * 4 + r] = x_ * sigmoidf_(x_); }
      const v4u hv = v4u{pk_bf16(s_[0], s_[1]), pk_bf16(s_[2], s_[3]), pk_bf16(s_[4], s_[5]), pk_bf16(s_[6], s_[7])};
      GST4_SC1(ho, hv, ws + i * 57344, 0);
    }
  }
  ISSUE_W2(W2A + W2B + W2C, 56);
  STAMP(4);
  BAR();
  {
    FRESH_IDS();
    TIE(fhi); TIE(flo);
    const int ch = tid >> 8, bi = tid & 255;
    if (bi == 255) flo = v2u{0u, 0u};
    const unsigned W2_ = flo.x, W3 = flo.y, W4 = fhi.x, W5 = fhi.y, W6 = fhi.z, W7 = fhi.w;
    const unsigned S3 = __builtin_amdgcn_alignbit(W3, W3, 16), S4 = __builtin_amdgcn_alignbit(W4, W4, 16), S5 = __builtin_amdgcn_alignbit(W5, W5, 16),
                   S6 = __builtin_amdgcn_alignbit(W6, W6, 16), S7 = __builtin_amdgcn_alignbit(W7, W7, 16);
    const unsigned T3 = (W3 & 0xffffu) | (W2_ & 0xffff0000u), T4 = (W4 & 0xffffu) | (W3 & 0xffff0000u), T5 = (W5 & 0xffffu) | (W4 & 0xffff0000u),
                   T6 = (W6 & 0xffffu) | (W5 & 0xffff0000u), T7 = (W7 & 0xffffu) | (W6 & 0xffff0000u);
    LAS char* fb = L + L_FILT + ch * 16640 + bi * 16;
    *(LAS v4u*)(fb) = v4u{S7, S6, S5, S4};
    *(LAS v4u*)(fb + 4160) = v4u{T7, T6, T5, T4};
    *(LAS v4u*)(fb + 8320) = v4u{S6, S5, S4, S3};
    *(LAS v4u*)(fb + 12480) = v4u{T6, T5, T4, T3};
    if (tid < 16) *(LAS v4u*)(L + L_FILT + (tid >> 1) * 4160 + 4096 + (tid & 1) * 16) = v4u{0u, 0u, 0u, 0u};
    else if (tid >= 64 && tid < 128 && (tid & 31) < 30) *(LAS v4u*)(L + L_U + ((tid >> 5) & 1) * 4608 + (tid & 31) * 16) = v4u{0u, 0u, 0u, 0u};
    if (g1) { WAITV(56 - W2A - W2B + 4); } else { WAITV(56 - W2A - W2B); }
    TIE(kvt);
    *(LAS v4u*)(L + L_U + (w >> 2) * 4608 + 480 + (w & 3) * 1024 + lane * 16) = kvt;
  }
  STAMP(5);
  WAITV_BAR(56 - W2A - W2B - W2C);
  if (threadIdx.x == 0 && g1) {
    const unsigned fo = (unsigned)(WS_FH + (R * 16 + c) * 8);
    GST2_SC1(fo, tagv, ws, 0);
  }
  STAMP(6);
  {
    FRESH_IDS();
    const int ch = w >> 2, mq = w & 3;
    f32x4 ca[8];
#pragma unroll
    for (int i = 0; i < 8; ++i) ca[i] = f32x4{0.f, 0.f, 0.f, 0.f};
    LAS char* const ab = L + L_FILT + ch * 16640 + (3 - (li & 3)) * 4160 + 2 * (2028 + 8 * g - 4 * (li >> 2)) - 512 * 7 - 128 * mq - 64;
    LAS char* const bb = L + L_U + ch * 4608 + 2 * (224 + 16 * li + 8 * g) - 128 * mq - 64;
#pragma unroll
    for (int mm = 0; mm < 2; ++mm) {
      v4u A[8], B[8];
#pragma unroll
      for (int i = 0; i < 8; ++i) {
        const v2u a0 = *(LAS v2u*)(ab + (1 - mm) * 64 + 512 * (7 - i));
        const v2u a1 = *(LAS v2u*)(ab + (1 - mm) * 64 + 512 * (7 - i) + 8);
        A[i] = v4u{a0.x, a0.y, a1.x, a1.y};
        B[i] = *(LAS v4u*)(bb + (1 - mm) * 64 + 512 * i);
      }
#pragma unroll
      for (int qq = 0; qq < 8; ++qq)
#pragma unroll
        for (int i = 0; i <= qq; ++i) ca[qq] = mfma16(A[i], B[qq - i], ca[qq]);
    }
#pragma unroll
    for (int qq = 0; qq < 8; ++qq) *(LAS f32x4*)(L + L_RED + (w * 8 + qq) * 1024 + lane * 16) = ca[qq];
  }
  BAR();
  STAMP(7);
  {
    FRESH_IDS();
    f32x4 c0 = f32x4{0.f, 0.f, 0.f, 0.f}, c1 = c0;
#pragma unroll
    for (int s = 0; s < 4; ++s) {
      c0 += *(LAS f32x4*)(L + L_RED + (s * 8 + w) * 1024 + lane * 16);
      c1 += *(LAS f32x4*)(L + L_RED + ((4 + s) * 8 + w) * 1024 + lane * 16);
    }
    const unsigned so = (unsigned)(256 * w + 16 * li + 4 * g);
    const v2u k0 = *(LAS v2u*)(L + L_U + 480 + so * 2), k1 = *(LAS v2u*)(L + L_U + 4608 + 480 + so * 2);
    const float D0 = blo(dd), D1 = bhi(dd);
    const v4u yv = v4u{pk_bf16(c0[0] + blo(k0.x) * D0, c1[0] + blo(k1.x) * D1), pk_bf16(c0[1] + bhi(k0.x) * D0, c1[1] + bhi(k1.x) * D1),
                       pk_bf16(c0[2] + blo(k0.y) * D0, c1[2] + blo(k1.y) * D1), pk_bf16(c0[3] + bhi(k0.y) * D0, c1[3] + bhi(k1.y) * D1)};
    const unsigned yo = (unsigned)(WS_Y2 + dpair * 8192 + so * 4);
    GST4_SC1(yo, yv, ws, 0);
  }
  {
    FRESH_IDS();
    const int c_lo = (7 * w) >> 2, c_hi = (7 * w + 6) >> 2;
    int cc = c_lo + (lane & 3);
    cc = cc > c_hi ? c_hi : cc;
    const unsigned ph = (unsigned)(WS_FH + ((st >> 1) * 16 + cc) * 8);
    v2u f;
    asm volatile("global_load_dwordx2 %0, %1, %2 sc1\n\ts_waitcnt vmcnt(0)" : "=&v"(f) : "v"(ph), "s"(ws) : "memory");
    spin_flags(f, ph, ws, tlo, thi);
  }
  STAMP(8);
  v4u hf[3][7];
  unsigned vh;
  { FRESH_IDS(); vh = (unsigned)(lane * 16); }
  const char* const hb = ws + WS_H + (size_t)(4 * st * 56 + 7 * w) * 1024;
#define ISSUE_H(slot, p)                                                                     \
  do {                                                                                       \
    const char* const hp_ = hb + ((((w >> 1) + 1 + (p)) & 3) * 57344) + 3072;                \
    _Pragma("unroll") for (int kl = 0; kl < 7; ++kl) GLD4_SC1(hf[slot][kl], vh, hp_, kl * 1024 - 3072); \
  } while (0)
  ISSUE_H(0, 0);
  ISSUE_H(1, 1);
  ISSUE_H(2, 2);
  BAR();
  if (threadIdx.x == 0) {
    const unsigned fo = (unsigned)(WS_FY + (n * 32 + st) * 8);
    GST2_SC1(fo, tagv, ws, 0);
  }
  v4u wf2[7][4];
#pragma unroll
  for (int m_ = 0; m_ < 56; ++m_) TIE(w2r[m_]);
#pragma unroll
  for (int kl = 0; kl < 7; ++kl)
#pragma unroll
    for (int e2 = 0; e2 < 4; ++e2) {
      const v2u a = w2r[kl * 8 + 2 * e2], b = w2r[kl * 8 + 2 * e2 + 1];
      wf2[kl][0][e2] = __builtin_amdgcn_perm(b.x, a.x, 0x05040100u);
      wf2[kl][1][e2] = __builtin_amdgcn_perm(b.x, a.x, 0x07060302u);
      wf2[kl][2][e2] = __builtin_amdgcn_perm(b.y, a.y, 0x05040100u);
      wf2[kl][3][e2] = __builtin_amdgcn_perm(b.y, a.y, 0x07060302u);
    }
  STAMP(9);
#define G2_TILE(slot, p)                                                                                    \
  do {                                                                                                      \
    _Pragma("unroll") for (int kl = 0; kl < 7; ++kl) TIE(hf[slot][kl]);                                     \
    f32x4 a2[4];                                                                                            \
    _Pragma("unroll") for (int j = 0; j < 4; ++j) a2[j] = f32x4{0.f, 0.f, 0.f, 0.f};                        \
    _Pragma("unroll") for (int kl = 0; kl < 7; ++kl)                                                        \
      _Pragma("unroll") for (int j = 0; j < 4; ++j) a2[j] = mfma16(wf2[kl][j], hf[slot][kl], a2[j]);        \
    LAS char* pb_ = L + w * 16384 + ((((w >> 1) + 1 + (p)) & 3) * 4096) + (vh >> 1);                         \
    _Pragma("unroll") for (int j = 0; j < 4; ++j) {                                                         \
      *(LAS f32x2*)(pb_ + j * 512) = f32x2{a2[j][0], a2[j][1]};                                             \
      *(LAS f32x2*)(pb_ + 2048 + j * 512) = f32x2{a2[j][2], a2[j][3]};                                      \
    }                                                                                                       \
  } while (0)
  WAITV(14);
  G2_TILE(0, 0);
  SCHED();
  v2u yf;
  unsigned py;
  { FRESH_IDS(); py = lane < 32 ? (unsigned)(WS_FY + (n * 32 + lane) * 8) : (unsigned)(WS_F0 + ((lane & 7) * 32 + st) * 8); }
  GLD2_SC1(yf, py, ws, 0);
  ISSUE_H(0, 3);
  WAITV(15);
  G2_TILE(1, 1);
  WAITV(7);
  TIE(yf);
  spin_flags(yf, py, ws, tlo, thi);
  unsigned yv[4];
  float gv[4];
  {
    FRESH_IDS();
    const unsigned yo = (unsigned)(WS_Y2 + (32 * n + 8 * g + 4 * (w & 1)) * 8192 + (64 * st + 16 * (w >> 1) + li) * 4);
#pragma unroll
    for (int i = 0; i < 4; ++i) GLD1_SC1(yv[i], yo, ws + i * 8192, 0);
    const unsigned go = (unsigned)(WS_GP + ((4 * st + (w >> 1)) * 16 + 4 * g) * 64 + li * 4);
#pragma unroll
    for (int i = 0; i < 4; ++i) GLD1_SC1(gv[i], go, ws, i * 64);
  }
  G2_TILE(2, 2);
  WAITV(8);
  G2_TILE(0, 3);
  STAMP(10);
  BAR();
  {
    FRESH_IDS();
    f32x2 b2[4];
#pragma unroll
    for (int j = 0; j < 4; ++j) b2[j] = f32x2{0.f, 0.f};
    LAS char* const pr = L + (w >> 1) * 4096 + (w & 1) * 2048 + lane * 8;
#pragma unroll
    for (int s = 0; s < 8; ++s)
#pragma unroll
      for (int j = 0; j < 4; ++j) b2[j] += *(LAS f32x2*)(pr + s * 16384 + j * 512);
    WAITV(0);
#pragma unroll
    for (int i = 0; i < 4; ++i) { TIE(yv[i]); TIE(gv[i]); }
    float lg = (gv[0] + gv[1]) + (gv[2] + gv[3]);
    lg += __shfl_xor(lg, 16);
    lg += __shfl_xor(lg, 32);
    const float gt = sigmoidf_(lg), og = 1.0f - gt;
    v4u o;
#pragma unroll
    for (int i = 0; i < 4; ++i) {
      const float b0 = b2[(2 * i) & 3][i >> 1], b1 = b2[(2 * i + 1) & 3][i >> 1];
      o[i] = pk_bf16(gt * (blo(qr[i]) * blo(yv[i])) + og * b0, gt * (bhi(qr[i]) * bhi(yv[i])) + og * b1);
    }
    const unsigned ooff = (unsigned)((64 * st + 16 * (w >> 1) + li) * 1024 + n * 128 + g * 32 + (w & 1) * 16);
    GST4(ooff, o, out, 0);
  }
}

__global__ __launch_bounds__(512, 2) void fused_kernel(const char* __restrict__ q, const char* __restrict__ k, const char* __restrict__ v,
                                                       const char* __restrict__ sk, const char* __restrict__ ssmk, const char* __restrict__ ssmD,
                                                       const char* __restrict__ w1, const char* __restrict__ w2, const char* __restrict__ gw,
                                                       char* out, char* ws) {
  if ((blockIdx.x >> 3) < 28) body<true>(q, k, v, sk, ssmk, ssmD, w1, w2, gw, out, ws);
  else body<false>(q, k, v, sk, ssmk, ssmD, w1, w2, gw, out, ws);
}

extern "C" void kernel_launch(void* const* d_in, const int* in_sizes, int n_in, void* d_out, int out_size, void* d_ws, size_t ws_size,
                              hipStream_t stream) {
  (void)in_sizes; (void)n_in; (void)out_size; (void)ws_size;
  (void)hipFuncSetAttribute((const void*)fused_kernel, hipFuncAttributeMaxDynamicSharedMemorySize, LDS_BYTES);
  fused_kernel<<<256, 512, LDS_BYTES, stream>>>((const char*)d_in[0], (const char*)d_in[1], (const char*)d_in[2], (const char*)d_in[3],
                                                (const char*)d_in[4], (const char*)d_in[5], (const char*)d_in[7], (const char*)d_in[8],
                                                (const char*)d_in[9], (char*)d_out, (char*)d_ws);
}
```

```cpp
#include <hip/hip_runtime.h>
#include <stdint.h>

typedef unsigned v4u __attribute__((ext_vector_type(4)));
typedef unsigned v2u __attribute__((ext_vector_type(2)));
typedef float f32x4 __attribute__((ext_vector_type(4)));
typedef float f32x2 __attribute__((ext_vector_type(2)));
typedef short bf16x8 __attribute__((ext_vector_type(8)));
typedef short s16x4 __attribute__((ext_vector_type(4)));
typedef __bf16 bf2 __attribute__((ext_vector_type(2)));
typedef unsigned long long u64;
#define LAS __attribute__((address_space(3)))

#define KSTAMP 0

constexpr unsigned WS_F0 = 4096;
constexpr unsigned WS_FH = 8192;
constexpr unsigned WS_FY = 12288;
constexpr unsigned WS_GP = 16384;
constexpr unsigned WS_TS = 262144;
constexpr unsigned WS_KVT = 1u << 20;
constexpr unsigned WS_Y2 = 3u << 20;
constexpr unsigned WS_W2T = 5u << 20;
constexpr unsigned WS_H = 7u << 20;

constexpr unsigned L_KB = 131072;
constexpr unsigned L_VB = 140288;
constexpr unsigned L_YT = 131072;
constexpr unsigned L_CNT = 148480;
constexpr unsigned L_FILT = 0;
constexpr unsigned L_U = 33280;
constexpr unsigned L_RED = 43008;
constexpr unsigned LDS_BYTES = 148544;

constexpr int SPIN_CAP = 1 << 16;

__device__ __forceinline__ unsigned pk_bf16(float a, float b) {
  f32x2 v = {a, b};
  return __builtin_bit_cast(unsigned, __builtin_convertvector(v, bf2));
}
__device__ __forceinline__ float blo(unsigned u) { return __builtin_bit_cast(float, u << 16); }
__device__ __forceinline__ float bhi(unsigned u) { return __builtin_bit_cast(float, u & 0xffff0000u); }
__device__ __forceinline__ f32x4 mfma16(v4u a, v4u b, f32x4 c) {
  return __builtin_amdgcn_mfma_f32_16x16x32_bf16(__builtin_bit_cast(bf16x8, a), __builtin_bit_cast(bf16x8, b), c, 0, 0, 0);
}
__device__ __forceinline__ float sigmoidf_(float x) {
  return __builtin_amdgcn_rcpf(1.0f + __builtin_amdgcn_exp2f(-1.4426950408889634f * x));
}
__device__ __forceinline__ float sum8(float x) {
  x += __builtin_bit_cast(float, __builtin_amdgcn_update_dpp(0, __builtin_bit_cast(int, x), 0xB1, 0xf, 0xf, true));
  x += __builtin_bit_cast(float, __builtin_amdgcn_update_dpp(0, __builtin_bit_cast(int, x), 0x4E, 0xf, 0xf, true));
  x += __builtin_bit_cast(float, __builtin_amdgcn_update_dpp(0, __builtin_bit_cast(int, x), 0x141, 0xf, 0xf, true));
  return x;
}

#define GLD1(dst, voff, sbase, imm) asm volatile("global_load_dword %0, %1, %2 offset:%3" : "=v"(dst) : "v"(voff), "s"(sbase), "i"(imm))
#define GLD1_SC1(dst, voff, sbase, imm) asm volatile("global_load_dword %0, %1, %2 offset:%3 sc1" : "=v"(dst) : "v"(voff), "s"(sbase), "i"(imm))
#define GLD2(dst, voff, sbase, imm) asm volatile("global_load_dwordx2 %0, %1, %2 offset:%3" : "=v"(dst) : "v"(voff), "s"(sbase), "i"(imm))
#define GLD2_SC1(dst, voff, sbase, imm) asm volatile("global_load_dwordx2 %0, %1, %2 offset:%3 sc1" : "=v"(dst) : "v"(voff), "s"(sbase), "i"(imm))
#define GLD4(dst, voff, sbase, imm) asm volatile("global_load_dwordx4 %0, %1, %2 offset:%3" : "=v"(dst) : "v"(voff), "s"(sbase), "i"(imm))
#define GLD4_SC1(dst, voff, sbase, imm) asm volatile("global_load_dwordx4 %0, %1, %2 offset:%3 sc1" : "=v"(dst) : "v"(voff), "s"(sbase), "i"(imm))
#define GST4_SC1(voff, data, sbase, imm) asm volatile("global_store_dwordx4 %0, %1, %2 offset:%3 sc1\n\ts_nop 1" ::"v"(voff), "v"(data), "s"(sbase), "i"(imm) : "memory")
#define GST4(voff, data, sbase, imm) asm volatile("global_store_dwordx4 %0, %1, %2 offset:%3\n\ts_nop 1" ::"v"(voff), "v"(data), "s"(sbase), "i"(imm) : "memory")
#define FLAG_ST(voff, data, sbase) asm volatile("global_store_dwordx2 %0, %1, %2 sc1 ; FLAG" ::"v"(voff), "v"(data), "s"(sbase) : "memory")
#define GST1_SC1(voff, data, sbase, imm) asm volatile("global_store_dword %0, %1, %2 offset:%3 sc1" ::"v"(voff), "v"(data), "s"(sbase), "i"(imm) : "memory")
#define DMA16(ldsaddr, voff, sbase) asm volatile("s_mov_b32 m0, %0\n\ts_nop 0\n\tglobal_load_lds_dwordx4 %1, %2" ::"s"(ldsaddr), "v"(voff), "s"(sbase) : "memory")
#define WAITV(n) asm volatile("s_waitcnt vmcnt(%0)" ::"i"(n) : "memory")
#define BAR() asm volatile("s_waitcnt lgkmcnt(0)\n\ts_barrier" ::: "memory")
#define TIE(x) asm volatile("" : "+v"(x))
#define SCHED() __builtin_amdgcn_sched_barrier(0)
#define FRESH_IDS()                                         \
  int tid = threadIdx.x;                                    \
  asm volatile("" : "+v"(tid));                             \
  const int lane = tid & 63, li = lane & 15, g = lane >> 4; \
  (void)li; (void)g;

#define STAMP(i) do {} while (0)

__device__ __forceinline__ bool all_eq(v2u f, unsigned tlo, unsigned thi) {
  return __builtin_amdgcn_ballot_w64(f.x == tlo && f.y == thi) == ~0ull;
}
__device__ __forceinline__ void spin_flags(v2u f, unsigned voff, const char* ws, unsigned tlo, unsigned thi) {
  int spins = 0;
  while (__builtin_expect(!all_eq(f, tlo, thi), 0)) {
    if (++spins > SPIN_CAP) break;
    __builtin_amdgcn_s_sleep(1);
    asm volatile("global_load_dwordx2 %0, %1, %2 sc1 ; SPIN\n\ts_waitcnt vmcnt(0) ; SPIN" : "=&v"(f) : "v"(voff), "s"(ws) : "memory");
  }
}

#define W2A 9
#define W2B 9
#define HWIN 12

template <bool g1, int ROLE>
__device__ __forceinline__ void body(const char* __restrict__ q, const char* __restrict__ k, const char* __restrict__ v,
                                     const char* __restrict__ sk, const char* __restrict__ ssmk, const char* __restrict__ ssmD,
                                     const char* __restrict__ w1, const char* __restrict__ w2, const char* __restrict__ gw,
                                     char* out, char* ws, const int w, unsigned cpf
) {
  extern __shared__ __attribute__((aligned(16))) char smem_[];
  LAS char* const L = (LAS char*)smem_;
  const unsigned lbase = (unsigned)(uintptr_t)L;
  constexpr bool LD = g1 && ROLE == 0;
  constexpr bool SD = ROLE == 1;
  const int id = blockIdx.x, xcd = id & 7, loc = id >> 3;
  const int st = 4 * xcd + (loc >> 3), n = loc & 7;
  const int R = 4 * (xcd >> 1) + (g1 ? loc / 7 : 0), c = 7 * (xcd & 1) + loc % 7;
  const int dpair = 32 * n + st;
  const int j4 = w & 3;

  unsigned vx0 = 0, vx1 = 0, vw0 = 0, vw1 = 0;
  const char* const xb = q + (size_t)R * 131072 + j4 * 32768;
  const char* const wb = w1 + c * 256 + j4 * 57344;
  if (LD) {
    FRESH_IDS();
    vx0 = (unsigned)((lane >> 3) * 1024 + (((lane & 7) ^ g) * 16));
    vx1 = (unsigned)((8 + (lane >> 3)) * 1024 + (((lane & 7) ^ (4 + g)) * 16));
    vw0 = (unsigned)(g * 3584 + (((li >> 1) ^ g) * 32) + (li & 1) * 16);
    vw1 = (unsigned)((8 + g) * 3584 + (((li >> 1) ^ (g | 4)) * 32) + (li & 1) * 16);
  }
#define DMA_PIECE(ch, p)                                                                                      \
  do {                                                                                                        \
    if (LD) {                                                                                                 \
      const unsigned sl_ = lbase + ((ch) & 3) * 32768 + j4 * 4096;                                            \
      if ((p) == 0) DMA16(sl_, vx0, xb + (ch) * 128);                                                         \
      if ((p) == 1) DMA16(sl_ + 16384, vw0, wb + (ch) * 229376);                                              \
      if ((p) == 2) DMA16(sl_ + 1024, vx1, xb + (ch) * 128);                                                  \
      if ((p) == 3) DMA16(sl_ + 16384 + 1024, vw0, wb + (ch) * 229376 + 14336);                               \
      if ((p) == 4) DMA16(sl_ + 2048, vx0, xb + (ch) * 128 + 16384);                                          \
      if ((p) == 5) DMA16(sl_ + 16384 + 2048, vw1, wb + (ch) * 229376);                                       \
      if ((p) == 6) DMA16(sl_ + 3072, vx1, xb + (ch) * 128 + 16384);                                          \
      if ((p) == 7) DMA16(sl_ + 16384 + 3072, vw1, wb + (ch) * 229376 + 14336);                               \
    }                                                                                                         \
  } while (0)
#pragma unroll
  for (int ch = 0; ch < 4; ++ch)
#pragma unroll
    for (int p = 0; p < 8; ++p) DMA_PIECE(ch, p);

  u64 epoch;
  unsigned dd;
  asm volatile("s_load_dwordx2 %0, %1, 0x0" : "=s"(epoch) : "s"(ws));
  asm volatile("s_load_dword %0, %1, 0x0" : "=s"(dd) : "s"(ssmD + dpair * 4));

  v4u skr, qa, qb, gwr;
  unsigned wr[8];
  const bool retile = st < 28;
  if (SD) {
    FRESH_IDS();
    {
      const int kap = retile ? 2 * st + (j4 >> 1) : 0;
      const char* const b_ = w2 + (size_t)kap * 32768 + n * 128 + (j4 & 1) * 64;
      const unsigned vo = (unsigned)(g * 4096 + li * 4);
#pragma unroll
      for (int e = 0; e < 8; ++e) GLD1(wr[e], vo, b_ + (e >> 2) * 16384, (e & 3) * 1024);
    }
    const unsigned ro = (unsigned)((lane >> 3) * 1024);
    const char* const kb_ = k + (size_t)st * 65536 + n * 128 + j4 * 16384;
    const char* const vb_ = v + (size_t)st * 65536 + n * 128 + j4 * 16384;
    const unsigned ka = ro + (((lane & 7) ^ ((2 * j4 + 1) & 7)) * 16), kc = ro + (((lane & 7) ^ ((2 * j4 + 2) & 7)) * 16);
    const unsigned va = ro + (((lane & 7) ^ (2 * j4)) * 16);
    DMA16(lbase + L_KB + 1024 + j4 * 2048, ka, kb_);
    DMA16(lbase + L_KB + 2048 + j4 * 2048, kc, kb_ + 8192);
    DMA16(lbase + L_VB + j4 * 2048, va, vb_);
    DMA16(lbase + L_VB + 1024 + j4 * 2048, ka, vb_ + 8192);
    if (j4 == 0) {
      const unsigned ha = ro + (lane & 7) * 16;
      DMA16(lbase + L_KB, ha, kb_ - (st > 0 ? 8192 : 0));
    }
    const unsigned qo = ro + (lane & 7) * 16;
    const char* const qb_ = q + (size_t)st * 65536 + n * 128 + j4 * 16384;
    GLD4(qa, qo, qb_, 0);
    GLD4(qb, qo, qb_ + 8192, 0);
    const unsigned go = (unsigned)((lane & 7) * 16);
    GLD4(gwr, go, gw + n * 128, 0);
    const unsigned so = (unsigned)((lane >> 3) * 16);
    GLD4(skr, so, sk + n * 512 + j4 * 128, 0);
  }
  STAMP(12);

  f32x4 acc1[2][4];
#pragma unroll
  for (int a = 0; a < 2; ++a)
#pragma unroll
    for (int b = 0; b < 4; ++b) acc1[a][b] = f32x4{0.f, 0.f, 0.f, 0.f};
  const int wsx = w & 1, wf = w >> 1;
  unsigned ax0 = 0, aw0 = 0;
  if (g1) {
    FRESH_IDS();
    ax0 = (unsigned)((wsx * 64 + li) * 128 + ((g ^ (li >> 1)) * 16));
    const unsigned sg = (unsigned)((li >> 2) | ((g & 1) << 2));
    aw0 = (unsigned)(16384 + (8 * g + (li >> 2)) * 256 + (((2 * wf) ^ sg) * 32) + (lane & 3) * 8);
  }
  v4u xf[2][2][4], wfr[2][2][2];
#define RD_GROUP(ch, gq)                                                                                                              \
  do {                                                                                                                                \
    LAS char* const sl_ = L + ((ch) & 3) * 32768;                                                                                     \
    const s16x4 a_ = __builtin_amdgcn_ds_read_tr16_b64_v4i16((LAS s16x4*)(sl_ + (aw0 ^ (((gq) & 1) * 32)) + ((gq) >> 1) * 8192));        \
    const s16x4 b_ = __builtin_amdgcn_ds_read_tr16_b64_v4i16((LAS s16x4*)(sl_ + (aw0 ^ (((gq) & 1) * 32)) + ((gq) >> 1) * 8192 + 1024)); \
    const v2u a2 = __builtin_bit_cast(v2u, a_), b2 = __builtin_bit_cast(v2u, b_);                                                     \
    wfr[(ch) & 1][(gq) >> 1][(gq) & 1] = v4u{a2.x, a2.y, b2.x, b2.y};                                                                 \
    xf[(ch) & 1][(gq) >> 1][2 * ((gq) & 1)] = *(LAS v4u*)(sl_ + (ax0 ^ (((gq) >> 1) * 64)) + (2 * ((gq) & 1)) * 2048);                \
    xf[(ch) & 1][(gq) >> 1][2 * ((gq) & 1) + 1] = *(LAS v4u*)(sl_ + (ax0 ^ (((gq) >> 1) * 64)) + (2 * ((gq) & 1) + 1) * 2048);        \
  } while (0)
#define MM_GROUP(ch, gq)                                                                                              \
  do {                                                                                                                \
    _Pragma("unroll") for (int ks = 0; ks < 2; ++ks)                                                                  \
      _Pragma("unroll") for (int t2 = 0; t2 < 2; ++t2)                                                                \
        acc1[t2][gq] = mfma16(wfr[(ch) & 1][ks][t2], xf[(ch) & 1][ks][gq], acc1[t2][gq]);                             \
  } while (0)

  v4u w2f[28];
  unsigned vl16;
  const char* const w2tb = ws + WS_W2T + (size_t)(n * 56 + 7 * w) * 4096 + 4096;
#define W2_LOAD(m) GLD4_SC1(w2f[m], vl16, w2tb + ((m) >> 3) * 8192, (((m) & 7) - 4) * 1024)

#define G1_ITER(ch, wlo, whi)                                                                        \
  do {                                                                                               \
    _Pragma("unroll") for (int gq = 0; gq < 4; ++gq) {                                               \
      if (g1) { RD_GROUP((ch) + 1, gq); SCHED(); }                                                   \
      if ((ch) + 4 < 8) { DMA_PIECE((ch) + 4, 2 * gq); }                                             \
      if (g1) { SCHED(); MM_GROUP(ch, gq); SCHED(); }                                                \
      if ((ch) + 4 < 8) { DMA_PIECE((ch) + 4, 2 * gq + 1); }                                         \
      _Pragma("unroll") for (int m_ = (wlo) + (((whi) - (wlo)) * gq) / 4; m_ < (wlo) + (((whi) - (wlo)) * (gq + 1)) / 4; ++m_) W2_LOAD(m_); \
      SCHED();                                                                                       \
    }                                                                                                \
  } while (0)

  if (LD) WAITV(24);
  BAR();
  asm volatile("" : "+s"(epoch), "+s"(dd));
  const u64 tag = epoch * 6364136223846793005ull + 1442695040888963407ull;
  const unsigned tlo = (unsigned)tag, thi = (unsigned)(tag >> 32);
  const v2u tagv = v2u{tlo, thi};
  STAMP(1);
  if (g1) {
#pragma unroll
    for (int gq = 0; gq < 4; ++gq) RD_GROUP(0, gq);
  }
  SCHED();
  if (LD) WAITV(16); else WAITV(0);
  BAR();
  TIE(cpf);
  G1_ITER(0, 0, 0);
  if (SD) {
    FRESH_IDS();
    TIE(skr); TIE(qa); TIE(qb); TIE(gwr);
#pragma unroll
    for (int e = 0; e < 8; ++e) TIE(wr[e]);
    if (retile) {
      v4u f0, f1;
#pragma unroll
      for (int e2 = 0; e2 < 4; ++e2) {
        f0[e2] = __builtin_amdgcn_perm(wr[2 * e2 + 1], wr[2 * e2], 0x05040100u);
        f1[e2] = __builtin_amdgcn_perm(wr[2 * e2 + 1], wr[2 * e2], 0x07060302u);
      }
      const unsigned wo = (unsigned)(lane * 16);
      char* const b_ = ws + WS_W2T + (size_t)(n * 56 + 2 * st) * 4096 + j4 * 2048;
      GST4_SC1(wo, f0, b_, 0);
      GST4_SC1(wo, f1, b_, 1024);
    }
    const int sg = lane & 7, dp = 8 * j4 + (lane >> 3);
    const unsigned cb = (unsigned)((dp & 3) * 4), ck = (unsigned)(dp >> 2);
    unsigned kk[11], vv[8];
#pragma unroll
    for (int i = 0; i < 3; ++i) kk[i] = *(LAS unsigned*)(L + L_KB + (8 * sg + 5 + i) * 128 + ((ck ^ sg) * 16) + cb);
#pragma unroll
    for (int i = 0; i < 8; ++i) kk[3 + i] = *(LAS unsigned*)(L + L_KB + (8 * sg + 8 + i) * 128 + ((ck ^ ((sg + 1) & 7)) * 16) + cb);
#pragma unroll
    for (int i = 0; i < 8; ++i) vv[i] = *(LAS unsigned*)(L + L_VB + (8 * sg + i) * 128 + ((ck ^ sg) * 16) + cb);
    if (st == 0 && sg == 0) { kk[0] = 0u; kk[1] = 0u; kk[2] = 0u; }
    v4u o0, o1;
    {
      const float t0 = blo(skr.x), t1 = bhi(skr.x), t2 = blo(skr.y), t3 = bhi(skr.y);
      float r_[8];
#pragma unroll
      for (int i = 0; i < 8; ++i) r_[i] = (blo(kk[i + 3]) * t0 + blo(kk[i + 2]) * t1 + blo(kk[i + 1]) * t2 + blo(kk[i]) * t3) * blo(vv[i]);
      o0 = v4u{pk_bf16(r_[0], r_[1]), pk_bf16(r_[2], r_[3]), pk_bf16(r_[4], r_[5]), pk_bf16(r_[6], r_[7])};
    }
    {
      const float t0 = blo(skr.z), t1 = bhi(skr.z), t2 = blo(skr.w), t3 = bhi(skr.w);
      float r_[8];
#pragma unroll
      for (int i = 0; i < 8; ++i) r_[i] = (bhi(kk[i + 3]) * t0 + bhi(kk[i + 2]) * t1 + bhi(kk[i + 1]) * t2 + bhi(kk[i]) * t3) * bhi(vv[i]);
      o1 = v4u{pk_bf16(r_[0], r_[1]), pk_bf16(r_[2], r_[3]), pk_bf16(r_[4], r_[5]), pk_bf16(r_[6], r_[7])};
    }
    const unsigned ko = (unsigned)(dp * 8192 + sg * 16);
    char* const kvb = ws + WS_KVT + (size_t)n * 262144 + st * 128;
    GST4_SC1(ko, o0, kvb, 0);
    GST4_SC1(ko, o1, kvb + 4096, 0);
    float ga = 0.f, gb = 0.f;
#pragma unroll
    for (int i = 0; i < 4; ++i) {
      ga += blo(qa[i]) * blo(gwr[i]) + bhi(qa[i]) * bhi(gwr[i]);
      gb += blo(qb[i]) * blo(gwr[i]) + bhi(qb[i]) * bhi(gwr[i]);
    }
    ga = sum8(ga);
    gb = sum8(gb);
    const float gs = (lane & 1) ? gb : ga;
    const unsigned gpo = (unsigned)(((lane >> 3) + (lane & 1) * 8) * 4);
    GST1_SC1(gpo, gs, ws + WS_GP + (st * 8 + n) * 256 + j4 * 64, 0);
    SCHED();
  }
  if (LD) WAITV(16);
  BAR();
  G1_ITER(1, 0, 0);
  if (LD) WAITV(16); else WAITV(0);
  BAR();
  if (SD && threadIdx.x == 256) {
    const unsigned fo = (unsigned)(WS_F0 + (n * 32 + st) * 8);
    FLAG_ST(fo, tagv, ws);
  }
  STAMP(2);
  G1_ITER(2, 0, 0);
  if (LD) WAITV(16);
  BAR();
  G1_ITER(3, 0, 0);
  if (LD) WAITV(16);
  BAR();
  v2u pf;
  unsigned pf0;
  if (SD) {
    FRESH_IDS();
    pf0 = (unsigned)(WS_F0 + (n * 32 + (lane & 31)) * 8);
    GLD2_SC1(pf, pf0, ws, 0);
  }
  G1_ITER(4, 0, 0);
  if (LD) WAITV(8);
  if (SD) {
    WAITV(0);
    TIE(pf);
    if (!g1 && id == 8 * 28 && w == 4) {
      FRESH_IDS();
#pragma unroll 1
      for (int part = 0; part < 4; ++part) {
        const unsigned eo = (unsigned)(WS_F0 + (part * 64 + lane) * 8);
        spin_flags(v2u{~tlo, 0u}, eo, ws, tlo, thi);
      }
      if (lane == 0) { const unsigned z = 0; FLAG_ST(z, tagv, ws); }
    }
    spin_flags(pf, pf0, ws, tlo, thi);
  }
  BAR();
  STAMP(13);
  v4u kvt, fhi;
  v2u flo;
  {
    FRESH_IDS();
    vl16 = (unsigned)(lane * 16);
    GLD4_SC1(kvt, vl16, ws + WS_KVT + (size_t)(2 * dpair) * 4096 + w * 1024, 0);
    const int bi = tid & 255;
    const unsigned fo = (unsigned)((255 - bi) * 16);
    const char* const fb_ = ssmk + (size_t)(2 * dpair + (w >> 2)) * 4096;
    GLD4(fhi, fo, fb_, 0);
    const unsigned fo2 = bi == 255 ? fo : fo - 8;
    GLD2(flo, fo2, fb_, 0);
  }
  G1_ITER(5, 0, W2A);
  if (LD) WAITV(3 + W2A);
  BAR();
  G1_ITER(6, W2A, W2A + W2B);
  BAR();
  STAMP(3);
  if (g1) {
    char* const hb_ = ws + WS_H + (size_t)((8 * R + 4 * wsx) * 56 + 4 * c + wf) * 1024;
    MM_GROUP(7, 0);
#pragma unroll
    for (int i = 0; i < 4; ++i) {
      SCHED();
      if (i == 0) MM_GROUP(7, 1);
      if (i == 1) MM_GROUP(7, 2);
      if (i == 2) MM_GROUP(7, 3);
      SCHED();
      float s_[8];
#pragma unroll
      for (int t2 = 0; t2 < 2; ++t2)
#pragma unroll
        for (int r = 0; r < 4; ++r) { const float x_ = acc1[t2][i][r]; s_[t2 * 4 + r] = x_ * sigmoidf_(x_); }
      const v4u hv = v4u{pk_bf16(s_[0], s_[1]), pk_bf16(s_[2], s_[3]), pk_bf16(s_[4], s_[5]), pk_bf16(s_[6], s_[7])};
      GST4_SC1(vl16, hv, hb_ + i * 57344, 0);
    }
  }
  STAMP(4);
  constexpr int W2C = 28 - W2A - W2B;
#pragma unroll
  for (int m_ = W2A + W2B; m_ < 28; ++m_) W2_LOAD(m_);
  {
    FRESH_IDS();
    WAITV(28 + (g1 ? 4 : 0));
    TIE(fhi); TIE(flo); TIE(kvt);
    const int ch = w >> 2, bi = tid & 255;
    if (bi == 255) flo = v2u{0u, 0u};
    const unsigned W2_ = flo.x, W3 = flo.y, W4 = fhi.x, W5 = fhi.y, W6 = fhi.z, W7 = fhi.w;
    const unsigned S3 = __builtin_amdgcn_alignbit(W3, W3, 16), S4 = __builtin_amdgcn_alignbit(W4, W4, 16), S5 = __builtin_amdgcn_alignbit(W5, W5, 16),
                   S6 = __builtin_amdgcn_alignbit(W6, W6, 16), S7 = __builtin_amdgcn_alignbit(W7, W7, 16);
    const unsigned T3 = (W3 & 0xffffu) | (W2_ & 0xffff0000u), T4 = (W4 & 0xffffu) | (W3 & 0xffff0000u), T5 = (W5 & 0xffffu) | (W4 & 0xffff0000u),
                   T6 = (W6 & 0xffffu) | (W5 & 0xffff0000u), T7 = (W7 & 0xffffu) | (W6 & 0xffff0000u);
    LAS char* fb = L + L_FILT + ch * 16640 + bi * 16;
    *(LAS v4u*)(fb) = v4u{S7, S6, S5, S4};
    *(LAS v4u*)(fb + 4160) = v4u{T7, T6, T5, T4};
    *(LAS v4u*)(fb + 8320) = v4u{S6, S5, S4, S3};
    *(LAS v4u*)(fb + 12480) = v4u{T6, T5, T4, T3};
    *(LAS v4u*)(L + L_U + ch * 4608 + 480 + j4 * 1024 + lane * 16) = kvt;
    if (j4 == 0) {
      const unsigned za = lane < 32 ? (unsigned)(L_U + ch * 4608 + (lane < 30 ? lane : 29) * 16)
                                    : (unsigned)(L_FILT + ch * 16640 + ((lane >> 1) & 3) * 4160 + 4096 + (lane & 1) * 16);
      *(LAS v4u*)(L + za) = v4u{0u, 0u, 0u, 0u};
    }
  }
  STAMP(5);
  WAITV(W2C);
  BAR();
  if (g1 && ROLE == 0 && threadIdx.x == 0) {
    const unsigned fo = (unsigned)(WS_FH + (R * 16 + c) * 8);
    FLAG_ST(fo, tagv, ws);
  }
  STAMP(6);
  {
    FRESH_IDS();
    const int ch = w >> 2, mq = j4;
    f32x4 ca[8];
#pragma unroll
    for (int i = 0; i < 8; ++i) ca[i] = f32x4{0.f, 0.f, 0.f, 0.f};
    LAS char* const ab = L + L_FILT + ch * 16640 + (3 - (li & 3)) * 4160 + 2 * (2028 + 8 * g - 4 * (li >> 2)) - 512 * 7 - 128 * mq - 64;
    LAS char* const bb = L + L_U + ch * 4608 + 2 * (224 + 16 * li + 8 * g) - 128 * mq - 64;
#define CV_A(dst, mm, i)                                                        \
  do {                                                                          \
    const v2u a0_ = *(LAS v2u*)(ab + (1 - (mm)) * 64 + 512 * (7 - (i)));        \
    const v2u a1_ = *(LAS v2u*)(ab + (1 - (mm)) * 64 + 512 * (7 - (i)) + 8);    \
    dst = v4u{a0_.x, a0_.y, a1_.x, a1_.y};                                      \
  } while (0)
#define CV_B(dst, mm, j) dst = *(LAS v4u*)(bb + (1 - (mm)) * 64 + 512 * (j))
    v4u A0[8], B0[8], A1[8], B1[8];
    CV_A(A0[0], 0, 0);
#pragma unroll
    for (int j = 0; j < 8; ++j) CV_B(B0[j], 0, j);
    CV_A(A0[1], 0, 1);
    CV_A(A0[2], 0, 2);
    SCHED();
#pragma unroll
    for (int i = 0; i < 8; ++i) {
      if (i + 3 < 8) CV_A(A0[i + 3], 0, i + 3);
      if (i >= 3) { CV_A(A1[i - 3], 1, i - 3); CV_B(B1[i - 3], 1, i - 3); }
      SCHED();
#pragma unroll
      for (int j = 0; j < 8 - i; ++j) ca[i + j] = mfma16(A0[i], B0[j], ca[i + j]);
      SCHED();
    }
    LAS char* const rb = L + L_RED + w * 8192 + lane * 16;
#pragma unroll
    for (int qq = 0; qq < 8; ++qq) {
      if (qq < 3) { CV_A(A1[qq + 5], 1, qq + 5); CV_B(B1[qq + 5], 1, qq + 5); }
      SCHED();
#pragma unroll
      for (int i = 0; i <= qq; ++i) ca[qq] = mfma16(A1[i], B1[qq - i], ca[qq]);
      if (qq > 0) *(LAS f32x4*)(rb + (qq - 1) * 1024) = ca[qq - 1];
      SCHED();
    }
    *(LAS f32x4*)(rb + 7 * 1024) = ca[7];
  }
  v2u hfl;
  unsigned ph;
  {
    FRESH_IDS();
    const int c_lo = (7 * w) >> 2, c_hi = (7 * w + 6) >> 2;
    int cc = c_lo + (lane & 3);
    cc = cc > c_hi ? c_hi : cc;
    ph = (unsigned)(WS_FH + ((st >> 1) * 16 + cc) * 8);
    GLD2_SC1(hfl, ph, ws, 0);
  }
  BAR();
  STAMP(7);
  {
    FRESH_IDS();
    f32x4 c0 = f32x4{0.f, 0.f, 0.f, 0.f}, c1 = c0;
#pragma unroll
    for (int s = 0; s < 4; ++s) {
      c0 += *(LAS f32x4*)(L + L_RED + (s * 8 + w) * 1024 + lane * 16);
      c1 += *(LAS f32x4*)(L + L_RED + ((4 + s) * 8 + w) * 1024 + lane * 16);
    }
    const unsigned so = (unsigned)(256 * w + 16 * li + 4 * g);
    const v2u k0 = *(LAS v2u*)(L + L_U + 480 + so * 2), k1 = *(LAS v2u*)(L + L_U + 4608 + 480 + so * 2);
    const float D0 = blo(dd), D1 = bhi(dd);
    const v4u yv = v4u{pk_bf16(c0[0] + blo(k0.x) * D0, c1[0] + blo(k1.x) * D1), pk_bf16(c0[1] + bhi(k0.x) * D0, c1[1] + bhi(k1.x) * D1),
                       pk_bf16(c0[2] + blo(k0.y) * D0, c1[2] + blo(k1.y) * D1), pk_bf16(c0[3] + bhi(k0.y) * D0, c1[3] + bhi(k1.y) * D1)};
    WAITV(0);
    TIE(hfl);
    const unsigned yo = so * 4;
    GST4_SC1(yo, yv, ws + WS_Y2 + (size_t)dpair * 8192, 0);
  }
  spin_flags(hfl, ph, ws, tlo, thi);
  STAMP(8);
#pragma unroll
  for (int m_ = 0; m_ < 28; ++m_) TIE(w2f[m_]);
  v4u hf[HWIN];
  const char* const hb = ws + WS_H + (size_t)(4 * st * 56 + 7 * w) * 1024 + 3072;
#define H_LOAD(x) GLD4_SC1(hf[(x) % HWIN], vl16, hb + ((((w >> 1) + 1 + (x) / 7) & 3) * 57344), ((x) % 7) * 1024 - 3072)
#pragma unroll
  for (int x = 0; x < HWIN; ++x) H_LOAD(x);
  WAITV(HWIN);
  BAR();
  if (ROLE == 0 && threadIdx.x == 0) {
    const unsigned fo = (unsigned)(WS_FY + (n * 32 + st) * 8);
    FLAG_ST(fo, tagv, ws);
  }
  STAMP(9);
  unsigned pw;
  { FRESH_IDS(); pw = (unsigned)(w * 16384 + g * 256 + ((li ^ (g << 2)) * 16)); }
  v2u yf;
  unsigned py;
  v4u yt, qr;
  float gv;
  constexpr int XP = 10;
  constexpr int XC = 28 - HWIN + 2;
  static_assert(XC - 1 >= XP + HWIN - 6, "");
  f32x4 a2[4];
#pragma unroll
  for (int x = 0; x < 28; ++x) {
    {
      const int last = (x - 1 + HWIN < 27) ? x - 1 + HWIN : 27;
      int yc = last - x;
      if (x > XP && x <= XP + HWIN) yc += 1;
      if (x > XC) yc += 3;
      WAITV(yc);
    }
    TIE(hf[x % HWIN]);
    if (x % 7 == 0) {
#pragma unroll
      for (int j = 0; j < 4; ++j) a2[j] = f32x4{0.f, 0.f, 0.f, 0.f};
    }
#pragma unroll
    for (int j = 0; j < 4; ++j) a2[j] = mfma16(w2f[(x % 7) * 4 + j], hf[x % HWIN], a2[j]);
    SCHED();
    if (x + HWIN < 28) H_LOAD(x + HWIN);
    if (x == XP) {
      FRESH_IDS();
      py = lane < 32 ? (unsigned)(WS_FY + (n * 32 + lane) * 8) : (unsigned)(WS_F0 + ((lane & 7) * 32 + st) * 8);
      GLD2_SC1(yf, py, ws, 0);
    }
    if (x == XC) {
      FRESH_IDS();
      WAITV(27 - (XP + HWIN));
      TIE(yf);
      spin_flags(yf, py, ws, tlo, thi);
      const unsigned yo = (unsigned)(g * 8192 + li * 16);
      GLD4_SC1(yt, yo, ws + WS_Y2 + (size_t)(32 * n + 4 * w) * 8192 + st * 256, 0);
      const unsigned go = (unsigned)((lane & 7) * 256 + (lane >> 3) * 4);
      GLD1_SC1(gv, go, ws + WS_GP + st * 2048 + w * 32, 0);
      const unsigned qo = (unsigned)((lane >> 3) * 1024 + (lane & 7) * 16);
      GLD4(qr, qo, q + (size_t)(64 * st + 8 * w) * 1024 + n * 128, 0);
    }
    if (x % 7 == 6) {
      LAS char* const pb_ = L + pw + ((((w >> 1) + 1 + x / 7) & 3) * 4096);
#pragma unroll
      for (int j = 0; j < 4; ++j) *(LAS f32x4*)(pb_ + j * 1024) = a2[j];
    }
    SCHED();
  }
  {
    FRESH_IDS();
    WAITV(2);
    TIE(yt);
    *(LAS v4u*)(L + L_YT + (4 * w + g) * 256 + w * 16 + li * 16) = yt;
  }
  STAMP(10);
  BAR();
  {
    FRESH_IDS();
    const int r8 = lane >> 3, c8 = lane & 7, gg = c8 & 3;
    f32x4 b0 = f32x4{0.f, 0.f, 0.f, 0.f}, b1 = b0;
    LAS char* const pr = L + (w >> 1) * 4096 + (c8 >> 2) * 2048 + gg * 256 + (((8 * (w & 1) + r8) ^ (gg << 2)) * 16);
#pragma unroll
    for (int s = 0; s < 8; ++s) {
      b0 += *(LAS f32x4*)(pr + s * 16384);
      b1 += *(LAS f32x4*)(pr + s * 16384 + 1024);
    }
    unsigned yv[4];
#pragma unroll
    for (int i = 0; i < 4; ++i) yv[i] = *(LAS unsigned*)(L + L_YT + c8 * 1040 + i * 256 + (8 * w + r8) * 4);
    WAITV(0);
    TIE(gv); TIE(qr);
    const float gt = sigmoidf_(sum8(gv)), og = 1.0f - gt;
    v4u o;
#pragma unroll
    for (int i = 0; i < 4; ++i)
      o[i] = pk_bf16(gt * (blo(qr[i]) * blo(yv[i])) + og * b0[i], gt * (bhi(qr[i]) * bhi(yv[i])) + og * b1[i]);
    const unsigned oo = (unsigned)(r8 * 1024 + c8 * 16);
    asm volatile(".Lpf_end%0:" ::"i"((g1 ? 2 : 0) + ROLE));
    GST4(oo, o, out + (size_t)(64 * st + 8 * w) * 1024 + n * 128, 0);
  }
}

#define CODE_PF 128

extern "C" __global__ __launch_bounds__(512, 2) void fused_kernel(const char* __restrict__ q, const char* __restrict__ k, const char* __restrict__ v,
                                                       const char* __restrict__ sk, const char* __restrict__ ssmk, const char* __restrict__ ssmD,
                                                       const char* __restrict__ w1, const char* __restrict__ w2, const char* __restrict__ gw,
                                                       char* out, char* ws) {
#define TE
  unsigned cpf = 0;
  {
    u64 pc;
    unsigned l0, l1, l2, l3;
    asm volatile("s_getpc_b64 %0\n.Lpf_here:\n\ts_mov_b32 %1, .Lpf_end0-.Lpf_here\n\ts_mov_b32 %2, .Lpf_end1-.Lpf_here\n\t"
                 "s_mov_b32 %3, .Lpf_end2-.Lpf_here\n\ts_mov_b32 %4, .Lpf_end3-.Lpf_here"
                 : "=s"(pc), "=s"(l0), "=s"(l1), "=s"(l2), "=s"(l3));
    l0 = l0 > l1 ? l0 : l1;
    l2 = l2 > l3 ? l2 : l3;
    const unsigned len = ((blockIdx.x >> 3) < 28 ? l2 : l0) & ~3u;
    unsigned off = threadIdx.x * CODE_PF;
    off = off < len ? off : len;
    GLD1(cpf, off, pc, 0);
  }
  const int w = __builtin_amdgcn_readfirstlane(threadIdx.x >> 6);
  if (__builtin_expect((blockIdx.x >> 3) < 28, 1)) {
    if (w < 4) body<true, 0>(q, k, v, sk, ssmk, ssmD, w1, w2, gw, out, ws, w, cpf TE);
    else body<true, 1>(q, k, v, sk, ssmk, ssmD, w1, w2, gw, out, ws, w, cpf TE);
  } else {
    if (w < 4) body<false, 0>(q, k, v, sk, ssmk, ssmD, w1, w2, gw, out, ws, w, cpf TE);
    else body<false, 1>(q, k, v, sk, ssmk, ssmD, w1, w2, gw, out, ws, w, cpf TE);
  }
}

extern "C" void kernel_launch(void* const* d_in, const int* in_sizes, int n_in, void* d_out, int out_size, void* d_ws, size_t ws_size,
                              hipStream_t stream) {
  (void)in_sizes; (void)n_in; (void)out_size; (void)ws_size;
  (void)hipFuncSetAttribute((const void*)fused_kernel, hipFuncAttributeMaxDynamicSharedMemorySize, LDS_BYTES);
  fused_kernel<<<256, 512, LDS_BYTES, stream>>>((const char*)d_in[0], (const char*)d_in[1], (const char*)d_in[2], (const char*)d_in[3],
                                                (const char*)d_in[4], (const char*)d_in[5], (const char*)d_in[7], (const char*)d_in[8],
                                                (const char*)d_in[9], (char*)d_out, (char*)d_ws);
}
```

```cpp
#include <hip/hip_runtime.h>
#include <stdint.h>

typedef unsigned v4u __attribute__((ext_vector_type(4)));
typedef unsigned v2u __attribute__((ext_vector_type(2)));
typedef float f32x4 __attribute__((ext_vector_type(4)));
typedef float f32x2 __attribute__((ext_vector_type(2)));
typedef short bf16x8 __attribute__((ext_vector_type(8)));
typedef short s16x4 __attribute__((ext_vector_type(4)));
typedef __bf16 bf2 __attribute__((ext_vector_type(2)));
typedef unsigned long long u64;
#define LAS __attribute__((address_space(3)))
#if defined(__HIP_DEVICE_COMPILE__) && !defined(ALLOW_PK)
#define NOPK __attribute__((target("no-packed-fp32-ops")))
#else
#define NOPK
#endif

#define KSTAMP 0
#define SLEEP 4

#define FSTR 128
constexpr unsigned WS_F0 = 786432;
constexpr unsigned WS_FH = 851968;
constexpr unsigned WS_FY = 917504;
constexpr unsigned WS_GP = 16384;
constexpr unsigned WS_TS = 262144;
constexpr unsigned WS_KVT = 1u << 20;
constexpr unsigned WS_Y2 = 3u << 20;
constexpr unsigned WS_H = 5u << 20;

constexpr unsigned L_KB = 131072;
constexpr unsigned L_VB = 140288;
constexpr unsigned L_YT = 131072;
constexpr unsigned L_CNT = 148480;
constexpr unsigned L_FILT = 0;
constexpr unsigned L_U = 33280;
constexpr unsigned L_RED = 65536;
constexpr unsigned LDS_BYTES = 148544;

constexpr int SPIN_CAP = 1 << 16;

NOPK __device__ __forceinline__ unsigned pk_bf16(float a, float b) {
  f32x2 v = {a, b};
  return __builtin_bit_cast(unsigned, __builtin_convertvector(v, bf2));
}
NOPK __device__ __forceinline__ float blo(unsigned u) { return __builtin_bit_cast(float, u << 16); }
NOPK __device__ __forceinline__ float bhi(unsigned u) { return __builtin_bit_cast(float, u & 0xffff0000u); }
NOPK __device__ __forceinline__ f32x4 mfma16(v4u a, v4u b, f32x4 c) {
  return __builtin_amdgcn_mfma_f32_16x16x32_bf16(__builtin_bit_cast(bf16x8, a), __builtin_bit_cast(bf16x8, b), c, 0, 0, 0);
}
NOPK __device__ __forceinline__ float sigmoidf_(float x) {
  return __builtin_amdgcn_rcpf(1.0f + __builtin_amdgcn_exp2f(-1.4426950408889634f * x));
}
NOPK __device__ __forceinline__ float silu1(float x) { return x * sigmoidf_(x); }
NOPK __device__ __forceinline__ float sum8(float x) {
  x += __builtin_bit_cast(float, __builtin_amdgcn_update_dpp(0, __builtin_bit_cast(int, x), 0xB1, 0xf, 0xf, true));
  x += __builtin_bit_cast(float, __builtin_amdgcn_update_dpp(0, __builtin_bit_cast(int, x), 0x4E, 0xf, 0xf, true));
  x += __builtin_bit_cast(float, __builtin_amdgcn_update_dpp(0, __builtin_bit_cast(int, x), 0x141, 0xf, 0xf, true));
  return x;
}

#define GLD1(dst, voff, sbase, imm) asm volatile("global_load_dword %0, %1, %2 offset:%3" : "=v"(dst) : "v"(voff), "s"(sbase), "i"(imm))
#define GLD1_SC1(dst, voff, sbase, imm) asm volatile("global_load_dword %0, %1, %2 offset:%3 sc1" : "=v"(dst) : "v"(voff), "s"(sbase), "i"(imm))
#define GLD2(dst, voff, sbase, imm) asm volatile("global_load_dwordx2 %0, %1, %2 offset:%3" : "=v"(dst) : "v"(voff), "s"(sbase), "i"(imm))
#define GLD2_SC1(dst, voff, sbase, imm) asm volatile("global_load_dwordx2 %0, %1, %2 offset:%3 sc1" : "=v"(dst) : "v"(voff), "s"(sbase), "i"(imm))
#define GLD4(dst, voff, sbase, imm) asm volatile("global_load_dwordx4 %0, %1, %2 offset:%3" : "=v"(dst) : "v"(voff), "s"(sbase), "i"(imm))
#define GLD4_SC1(dst, voff, sbase, imm) asm volatile("global_load_dwordx4 %0, %1, %2 offset:%3 sc1" : "=v"(dst) : "v"(voff), "s"(sbase), "i"(imm))
#define GST4_SC1(voff, data, sbase, imm) asm volatile("global_store_dwordx4 %0, %1, %2 offset:%3 sc1\n\ts_nop 1" ::"v"(voff), "v"(data), "s"(sbase), "i"(imm) : "memory")
#define GST4(voff, data, sbase, imm) asm volatile("global_store_dwordx4 %0, %1, %2 offset:%3\n\ts_nop 1" ::"v"(voff), "v"(data), "s"(sbase), "i"(imm) : "memory")
#define FLAG_ST(voff, data, sbase) asm volatile("global_store_dwordx2 %0, %1, %2 sc1 ; FLAG" ::"v"(voff), "v"(data), "s"(sbase) : "memory")
#define GST1_SC1(voff, data, sbase, imm) asm volatile("global_store_dword %0, %1, %2 offset:%3 sc1" ::"v"(voff), "v"(data), "s"(sbase), "i"(imm) : "memory")
#define DMA16(ldsaddr, voff, sbase) asm volatile("s_mov_b32 m0, %0\n\ts_nop 0\n\tglobal_load_lds_dwordx4 %1, %2" ::"s"(ldsaddr), "v"(voff), "s"(sbase) : "memory")
#define WAITV(n) asm volatile("s_waitcnt vmcnt(%0)" ::"i"(n) : "memory")
#define BAR() asm volatile("s_waitcnt lgkmcnt(0)\n\ts_barrier" ::: "memory")
#define TIE(x) asm volatile("" : "+v"(x))
#define SCHED() __builtin_amdgcn_sched_barrier(0)
#define FRESH_IDS()                                         \
  int tid = threadIdx.x;                                    \
  asm volatile("" : "+v"(tid));                             \
  const int lane = tid & 63, li = lane & 15, g = lane >> 4; \
  (void)li; (void)g;

#define STAMP(i) do {} while (0)

#define NOSYNC 0
NOPK __device__ __forceinline__ bool all_eq(v2u f, unsigned tlo, unsigned thi) {
  if (NOSYNC) return true;
  return __builtin_amdgcn_ballot_w64(f.x == tlo && f.y == thi) == ~0ull;
}
NOPK __device__ __forceinline__ void spin_flags(v2u f, unsigned voff, const char* ws, unsigned tlo, unsigned thi) {
  int spins = 0;
  while (__builtin_expect(!all_eq(f, tlo, thi), 0)) {
    if (++spins > SPIN_CAP) break;
    __builtin_amdgcn_s_sleep(SLEEP);
    asm volatile("global_load_dwordx2 %0, %1, %2 sc1 ; SPIN\n\ts_waitcnt vmcnt(0) ; SPIN" : "=&v"(f) : "v"(voff), "s"(ws) : "memory");
  }
}

#define HWIN 8
#define XPOLL 14
#define XCHK 20
#define DPRE_A 12
#define F0_IT 2
#define W2S 4
#define W2D 32
NOPK __host__ __device__ __forceinline__ constexpr int g2_issued(int x) {
  int c = HWIN;
  for (int t = 0; t < x; ++t) c += (t + HWIN < 28 ? 1 : 0) + (t == XPOLL ? 1 : 0) + (t == XCHK ? 3 : 0);
  return c;
}
NOPK __host__ __device__ __forceinline__ constexpr int g2_pos_h(int j) { return j < HWIN ? j : g2_issued(j - HWIN); }
NOPK __host__ __device__ __forceinline__ constexpr int g2_wait(int x) { return g2_issued(x) - g2_pos_h(x) - 1; }
NOPK __host__ __device__ __forceinline__ constexpr int g2_wait_poll() { return (g2_issued(XCHK) + (XCHK + HWIN < 28 ? 1 : 0)) - (g2_issued(XPOLL) + (XPOLL + HWIN < 28 ? 1 : 0)) - 1; }
template <bool g1, int ROLE>
NOPK __device__ __forceinline__ void body(const char* __restrict__ q, const char* __restrict__ k, const char* __restrict__ v,
                                     const char* __restrict__ sk, const char* __restrict__ ssmk, const char* __restrict__ ssmD,
                                     const char* __restrict__ w1, const char* __restrict__ w2, const char* __restrict__ gw,
                                     char* out, char* ws, const int w, unsigned cpf
) {
  extern __shared__ __attribute__((aligned(16))) char smem_[];
  LAS char* const L = (LAS char*)smem_;
  const unsigned lbase = (unsigned)(uintptr_t)L;
  constexpr bool CW = ROLE == 0, HW = ROLE == 1;
  const int id = blockIdx.x, xcd = id & 7, loc = id >> 3;
  const int st = 4 * xcd + (loc >> 3), n = loc & 7;
  const int R = 4 * (xcd >> 1) + (g1 ? loc / 7 : 0), c = 7 * (xcd & 1) + loc % 7;
  const int dpair = 32 * n + st;
  const int j4 = w & 3;

  if (HW) __builtin_amdgcn_s_setprio(3);
  constexpr bool G1C = g1 && CW;
  int st8 = st * 8;
  asm volatile("" : "+s"(st8));
  unsigned vx0 = 0, vx1 = 0, vw0 = 0, vw1 = 0;
  const char* const xb = q + (size_t)R * 131072 + j4 * 32768;
  const char* const wb = w1 + c * 256 + j4 * 57344;
  if (G1C) {
    FRESH_IDS();
    vx0 = (unsigned)((lane >> 3) * 1024 + (((lane & 7) ^ g) * 16));
    vx1 = (unsigned)((8 + (lane >> 3)) * 1024 + (((lane & 7) ^ (4 + g)) * 16));
    vw0 = (unsigned)(g * 3584 + (((li >> 1) ^ g) * 32) + (li & 1) * 16);
    vw1 = (unsigned)((8 + g) * 3584 + (((li >> 1) ^ (g | 4)) * 32) + (li & 1) * 16);
  }
#define ROTK 0
  const int rot = ROTK;
#define DMA_PIECE(ch, p)                                                                                      \
  do {                                                                                                        \
    if (G1C) {                                                                                                \
      const unsigned sl_ = lbase + ((ch) & 3) * 32768 + j4 * 4096;                                            \
      const int kc_ = ((ch) + rot) & 7;                                                                       \
      if ((p) == 0) DMA16(sl_, vx0, xb + kc_ * 128);                                                          \
      if ((p) == 1) DMA16(sl_ + 16384, vw0, wb + kc_ * 229376);                                               \
      if ((p) == 2) DMA16(sl_ + 1024, vx1, xb + kc_ * 128);                                                   \
      if ((p) == 3) DMA16(sl_ + 16384 + 1024, vw0, wb + kc_ * 229376 + 14336);                                \
      if ((p) == 4) DMA16(sl_ + 2048, vx0, xb + kc_ * 128 + 16384);                                           \
      if ((p) == 5) DMA16(sl_ + 16384 + 2048, vw1, wb + kc_ * 229376);                                        \
      if ((p) == 6) DMA16(sl_ + 3072, vx1, xb + kc_ * 128 + 16384);                                           \
      if ((p) == 7) DMA16(sl_ + 16384 + 3072, vw1, wb + kc_ * 229376 + 14336);                                \
    }                                                                                                         \
  } while (0)
#define DMA_RANGE(lo, hi) _Pragma("unroll") for (int d_ = (lo); d_ < (hi); ++d_) DMA_PIECE(d_ >> 3, d_ & 7)
  DMA_RANGE(0, DPRE_A);

  u64 epoch;
  unsigned dd;
  asm volatile("s_load_dwordx2 %0, %1, 0x0" : "=s"(epoch) : "s"(ws));
  asm volatile("s_load_dword %0, %1, 0x0" : "=s"(dd) : "s"(ssmD + dpair * 4));

  unsigned vl16;
  { FRESH_IDS(); vl16 = (unsigned)(lane * 16); }
  v4u fhi[2], skr, qa, qb, gwr;
  v2u flo[2];
  if (HW) {
    FRESH_IDS();
    const unsigned ro = (unsigned)((lane >> 3) * 1024);
    const char* const kb_ = k + (size_t)st * 65536 + n * 128 + j4 * 16384;
    const char* const vb_ = v + (size_t)st * 65536 + n * 128 + j4 * 16384;
    const unsigned ka = ro + (((lane & 7) ^ ((2 * j4 + 1) & 7)) * 16), kc = ro + (((lane & 7) ^ ((2 * j4 + 2) & 7)) * 16);
    const unsigned va = ro + (((lane & 7) ^ (2 * j4)) * 16);
    const unsigned qo = ro + (lane & 7) * 16;
    DMA16(lbase + L_KB + 1024 + j4 * 2048, ka, kb_);
    DMA16(lbase + L_KB + 2048 + j4 * 2048, kc, kb_ + 8192);
    DMA16(lbase + L_VB + j4 * 2048, va, vb_);
    DMA16(lbase + L_VB + 1024 + j4 * 2048, ka, vb_ + 8192);
    if (j4 == 0) DMA16(lbase + L_KB, qo, kb_ - (st > 0 ? 8192 : 0));
    const unsigned so = (unsigned)((lane >> 3) * 16);
    GLD4(skr, so, sk + n * 512 + j4 * 128, 0);
    const char* const qb_ = q + (size_t)st * 65536 + n * 128 + j4 * 16384;
    GLD4(qa, qo, qb_, 0);
    GLD4(qb, qo, qb_ + 8192, 0);
    const unsigned go = (unsigned)((lane & 7) * 16);
    GLD4(gwr, go, gw + n * 128, 0);
    if (tid == 256) *(LAS v4u*)(L + L_CNT) = v4u{0u, 0u, 0u, 0u};
    STAMP(1);
    WAITV(3);
  }
  if (CW) STAMP(1);
  BAR();
  STAMP(2);
  asm volatile("" : "+s"(epoch), "+s"(dd));
  const u64 tag = epoch * 6364136223846793005ull + 1442695040888963407ull;
  const unsigned tlo = (unsigned)tag, thi = (unsigned)(tag >> 32);
  const v2u tagv = v2u{tlo, thi};
  DMA_RANGE(DPRE_A, 24);
  if (G1C) WAITV(16);
#define KV_HALF(SEL, ta, tb, off)                                                                                          \
  do {                                                                                                                    \
    float kf[11], r_[8];                                                                                                  \
    const float t0 = blo(ta), t1 = bhi(ta), t2 = blo(tb), t3 = bhi(tb);                                                   \
    _Pragma("unroll") for (int i = 0; i < 11; ++i) kf[i] = SEL(kk[i]);                                                    \
    _Pragma("unroll") for (int i = 0; i < 8; ++i) r_[i] = (kf[i + 3] * t0 + kf[i + 2] * t1 + kf[i + 1] * t2 + kf[i] * t3) * SEL(vv[i]); \
    const v4u o_ = v4u{pk_bf16(r_[0], r_[1]), pk_bf16(r_[2], r_[3]), pk_bf16(r_[4], r_[5]), pk_bf16(r_[6], r_[7])};        \
    GST4_SC1(ko, o_, ws + WS_KVT + (size_t)n * 262144 + st * 128 + (off), 0);                                             \
  } while (0)
  if (HW) {
    FRESH_IDS();
    TIE(skr);
    const int sg = lane & 7, dp = 8 * j4 + (lane >> 3);
    const unsigned cb = (unsigned)((dp & 3) * 4), ck = (unsigned)(dp >> 2);
    unsigned kk[11], vv[8];
#pragma unroll
    for (int i = 0; i < 3; ++i) kk[i] = *(LAS unsigned*)(L + L_KB + (8 * sg + 5 + i) * 128 + ((ck ^ sg) * 16) + cb);
#pragma unroll
    for (int i = 0; i < 8; ++i) kk[3 + i] = *(LAS unsigned*)(L + L_KB + (8 * sg + 8 + i) * 128 + ((ck ^ ((sg + 1) & 7)) * 16) + cb);
#pragma unroll
    for (int i = 0; i < 8; ++i) vv[i] = *(LAS unsigned*)(L + L_VB + (8 * sg + i) * 128 + ((ck ^ sg) * 16) + cb);
    if (st == 0 && sg == 0) { kk[0] = 0u; kk[1] = 0u; kk[2] = 0u; }
    const unsigned ko = (unsigned)(dp * 8192 + sg * 16);
    KV_HALF(blo, skr.x, skr.y, 0);
    KV_HALF(bhi, skr.z, skr.w, 4096);
    SCHED();
  }
  if (!g1 && CW) WAITV(0);
  BAR();
  STAMP(3);
  TIE(cpf);
  if (HW) {
    FRESH_IDS();
    WAITV(2);
    TIE(qa); TIE(qb); TIE(gwr);
    float ga = 0.f, gb = 0.f;
#pragma unroll
    for (int i = 0; i < 4; ++i) {
      ga += blo(qa[i]) * blo(gwr[i]) + bhi(qa[i]) * bhi(gwr[i]);
      gb += blo(qb[i]) * blo(gwr[i]) + bhi(qb[i]) * bhi(gwr[i]);
    }
    ga = sum8(ga);
    gb = sum8(gb);
    const float gs = (lane & 1) ? gb : ga;
    const unsigned gpo = (unsigned)(((lane >> 3) + (lane & 1) * 8) * 4);
    GST1_SC1(gpo, gs, ws + WS_GP + (st8 + n) * 256 + j4 * 64, 0);
    __builtin_amdgcn_s_setprio(0);
    SCHED();
  }

  f32x4 acc1[4][4];
#pragma unroll
  for (int a = 0; a < 4; ++a)
#pragma unroll
    for (int b = 0; b < 4; ++b) acc1[a][b] = f32x4{0.f, 0.f, 0.f, 0.f};
  const int wsx = w & 1, wfx = (w >> 1) & 1;
  unsigned ax0 = 0, aw0 = 0;
  if (G1C) {
    FRESH_IDS();
    __builtin_amdgcn_s_setprio(1);
    ax0 = (unsigned)((wsx * 64 + li) * 128 + ((g ^ (li >> 1)) * 16));
    const unsigned sg = (unsigned)((li >> 2) | ((g & 1) << 2));
    aw0 = (unsigned)(16384 + (8 * g + (li >> 2)) * 256 + (((4 * wfx) ^ sg) * 32) + (lane & 3) * 8);
  }
  v4u xf[2][4];
  v2u wfa[2][4], wfb[2][4];
#define RD_WA(hs, gq) wfa[(hs) & 1][gq] = __builtin_bit_cast(v2u, __builtin_amdgcn_ds_read_tr16_b64_v4i16((LAS s16x4*)(L + (((hs) >> 1) & 3) * 32768 + (aw0 ^ ((gq) * 32)) + ((hs) & 1) * 8192)))
#define RD_WB(hs, gq) wfb[(hs) & 1][gq] = __builtin_bit_cast(v2u, __builtin_amdgcn_ds_read_tr16_b64_v4i16((LAS s16x4*)(L + (((hs) >> 1) & 3) * 32768 + (aw0 ^ ((gq) * 32)) + ((hs) & 1) * 8192 + 1024)))
#define RD_X(hs, gq) xf[(hs) & 1][gq] = *(LAS v4u*)(L + (((hs) >> 1) & 3) * 32768 + (ax0 ^ (((hs) & 1) * 64)) + (gq) * 2048)
#define MM1(hs, t, gq) acc1[t][gq] = mfma16(v4u{wfa[(hs) & 1][t].x, wfa[(hs) & 1][t].y, wfb[(hs) & 1][t].x, wfb[(hs) & 1][t].y}, xf[(hs) & 1][gq], acc1[t][gq])
#define MM_GROUP(hs, gq) _Pragma("unroll") for (int t_ = 0; t_ < 4; ++t_) MM1(hs, t_, gq)

  unsigned w2r[112], vw2;
  { FRESH_IDS(); vw2 = (unsigned)(g * 4096 + li * 4); }
  const char* const w2b = w2 + (size_t)w * 229376 + n * 128;
#define W2_LOAD(m) GLD1(w2r[m], vw2, w2b + ((m) >> 3) * 16384, (((m) >> 1) & 3) * 1024 + ((m) & 1) * 64)
#define W2_RANGE(lo, hi) _Pragma("unroll") for (int m_ = (lo); m_ < (hi); ++m_) { if (m_ >= 40 && (m_ & 3) == 0) WAITV(56); W2_LOAD(m_); }

#define HALF_STEP(hs, dlo, dhi)                                                                      \
  do {                                                                                               \
    if (G1C) {                                                                                       \
      _Pragma("unroll") for (int u_ = 0; u_ < 16; ++u_) {                                            \
        MM1(hs, u_ & 3, u_ >> 2);                                                                    \
        SCHED();                                                                                     \
        if (u_ == 0) RD_X((hs) + 1, 0);                                                              \
        else if (u_ < 9) { if (u_ & 1) RD_WA((hs) + 1, (u_ - 1) >> 1); else RD_WB((hs) + 1, (u_ - 1) >> 1); } \
        else if (u_ < 12) RD_X((hs) + 1, u_ - 8);                                                    \
        if ((u_ & 3) == 3) { DMA_RANGE((dlo) + (((dhi) - (dlo)) * (u_ >> 2)) / 4, (dlo) + (((dhi) - (dlo)) * ((u_ >> 2) + 1)) / 4); } \
        SCHED();                                                                                     \
      }                                                                                              \
    }                                                                                                \
  } while (0)
#define PIN_ACC1() do { if (G1C) { _Pragma("unroll") for (int a_ = 0; a_ < 16; ++a_) TIE(acc1[a_ & 3][a_ >> 2]); } } while (0)
  v2u pf;
  unsigned pf0;
  v4u kvt[2];
#define G1_HEAD(it, ncw)                                                                             \
  do {                                                                                               \
    PIN_ACC1();                                                                                      \
    if (G1C) WAITV(ncw);                                                                             \
    BAR();                                                                                           \
    STAMP(4 + (it));                                                                                 \
  } while (0)
#define F0_FLAG()                                                          \
  do {                                                                     \
    if (HW && threadIdx.x == 256) {                                        \
      const unsigned fo = (unsigned)(WS_F0 + (n * 32 + st) * FSTR);        \
      FLAG_ST(fo, tagv, ws);                                               \
    }                                                                      \
  } while (0)
  if (G1C) {
#pragma unroll
    for (int gq = 0; gq < 4; ++gq) { RD_WA(0, gq); RD_WB(0, gq); RD_X(0, gq); }
    SCHED();
  }
  HALF_STEP(0, 24, 32);
  G1_HEAD(0, 16);
  HALF_STEP(1, 32, 36); HALF_STEP(2, 36, 40);
  if (HW && F0_IT == 1) WAITV(0);
  G1_HEAD(1, 16);
  if (F0_IT == 1) F0_FLAG();
  HALF_STEP(3, 40, 44); HALF_STEP(4, 44, 48);
  if (HW && F0_IT == 2) WAITV(0);
  G1_HEAD(2, 16);
  if (F0_IT == 2) F0_FLAG();
  HALF_STEP(5, 48, 52); HALF_STEP(6, 52, 56);
  G1_HEAD(3, 16);
  if (HW) {
    FRESH_IDS();
    const int bi = tid & 255;
    const unsigned fo = (unsigned)((255 - bi) * 16);
    const unsigned fo2 = bi == 255 ? fo : fo - 8;
    const char* const fb_ = ssmk + (size_t)(2 * dpair) * 4096;
    GLD4(fhi[0], fo, fb_, 0);
    GLD2(flo[0], fo2, fb_, 0);
    GLD4(fhi[1], fo, fb_ + 4096, 0);
    GLD2(flo[1], fo2, fb_ + 4096, 0);
  }
  HALF_STEP(7, 56, 60); HALF_STEP(8, 60, 64);
  G1_HEAD(4, 16);
  HALF_STEP(9, 0, 0); HALF_STEP(10, 0, 0);
  if (HW) { W2_RANGE(0, 28); }
  G1_HEAD(5, 8);
  HALF_STEP(11, 0, 0); HALF_STEP(12, 0, 0);
  if (HW) {
    FRESH_IDS();
    pf0 = (unsigned)(WS_F0 + (n * 32 + 8 * j4 + (lane & 7)) * FSTR);
    GLD2_SC1(pf, pf0, ws, 0);
    W2_RANGE(28, 56);
  }
  G1_HEAD(6, 0);
  HALF_STEP(13, 0, 0); HALF_STEP(14, 0, 0);
  STAMP(11);

#define COUNT_LAST(idx, last) \
  (__builtin_amdgcn_readfirstlane((threadIdx.x & 63) == 0 ? __hip_atomic_fetch_add((LAS unsigned*)(L + L_CNT + 4 * (idx)), 1u, __ATOMIC_RELAXED, __HIP_MEMORY_SCOPE_WORKGROUP) : 0u) == (last))
  if (CW) {
    char* const hb_ = ws + WS_H + (size_t)((8 * R + 4 * wsx) * 56 + 4 * c + 2 * wfx) * 1024;
#define SILU_TILE(i, kp)                                                                                               \
  do {                                                                                                                 \
    if (g1) {                                                                                                          \
    float s_[8];                                                                                                       \
    _Pragma("unroll") for (int t2 = 0; t2 < 2; ++t2)                                                                   \
      _Pragma("unroll") for (int r = 0; r < 4; ++r) s_[t2 * 4 + r] = silu1(acc1[2 * (kp) + t2][i][r]);                 \
    const v4u hv = v4u{pk_bf16(s_[0], s_[1]), pk_bf16(s_[2], s_[3]), pk_bf16(s_[4], s_[5]), pk_bf16(s_[6], s_[7])};    \
    GST4_SC1(vl16, hv, hb_ + (i) * 57344, (kp) * 1024);                                                                \
    }                                                                                                                  \
    SCHED();                                                                                                           \
  } while (0)
    if (g1) { MM_GROUP(15, 0); SCHED(); MM_GROUP(15, 1); SCHED(); }
    SILU_TILE(0, 0); W2_RANGE(0 * W2S, 1 * W2S); SILU_TILE(0, 1); W2_RANGE(1 * W2S, 2 * W2S);
    if (g1) { SCHED(); MM_GROUP(15, 2); SCHED(); }
    SILU_TILE(1, 0); W2_RANGE(2 * W2S, 3 * W2S); SILU_TILE(1, 1); W2_RANGE(3 * W2S, 4 * W2S);
    if (g1) { SCHED(); MM_GROUP(15, 3); SCHED(); }
    SILU_TILE(2, 0); W2_RANGE(4 * W2S, 5 * W2S); SILU_TILE(2, 1); W2_RANGE(5 * W2S, 6 * W2S);
    SCHED();
    SILU_TILE(3, 0); W2_RANGE(6 * W2S, 7 * W2S); SILU_TILE(3, 1);
    __builtin_amdgcn_s_setprio(0);
    STAMP(12);
  } else {
    FRESH_IDS();
    WAITV(28);
    TIE(pf);
    spin_flags(pf, pf0, ws, tlo, thi);
    STAMP(26);
    GLD4_SC1(kvt[0], vl16, ws + WS_KVT + (size_t)(2 * dpair) * 4096 + j4 * 1024, 0);
    GLD4_SC1(kvt[1], vl16, ws + WS_KVT + (size_t)(2 * dpair + 1) * 4096 + j4 * 1024, 0);
    W2_RANGE(56, 84);
    const int bi = tid & 255;
#pragma unroll
    for (int ch = 0; ch < 2; ++ch) {
      TIE(fhi[ch]); TIE(flo[ch]);
      if (bi == 255) flo[ch] = v2u{0u, 0u};
      const unsigned W2_ = flo[ch].x, W3 = flo[ch].y, W4 = fhi[ch].x, W5 = fhi[ch].y, W6 = fhi[ch].z, W7 = fhi[ch].w;
      const unsigned S3 = __builtin_amdgcn_alignbit(W3, W3, 16), S4 = __builtin_amdgcn_alignbit(W4, W4, 16), S5 = __builtin_amdgcn_alignbit(W5, W5, 16),
                     S6 = __builtin_amdgcn_alignbit(W6, W6, 16), S7 = __builtin_amdgcn_alignbit(W7, W7, 16);
      const unsigned T3 = (W3 & 0xffffu) | (W2_ & 0xffff0000u), T4 = (W4 & 0xffffu) | (W3 & 0xffff0000u), T5 = (W5 & 0xffffu) | (W4 & 0xffff0000u),
                     T6 = (W6 & 0xffffu) | (W5 & 0xffff0000u), T7 = (W7 & 0xffffu) | (W6 & 0xffff0000u);
      LAS char* fb = L + L_FILT + ch * 16640 + bi * 16;
      *(LAS v4u*)(fb) = v4u{S7, S6, S5, S4};
      *(LAS v4u*)(fb + 4160) = v4u{T7, T6, T5, T4};
      *(LAS v4u*)(fb + 8320) = v4u{S6, S5, S4, S3};
      *(LAS v4u*)(fb + 12480) = v4u{T6, T5, T4, T3};
    }
    if (j4 < 2) {
      const unsigned za = lane < 32 ? (unsigned)(L_U + j4 * 4608 + (lane < 30 ? lane : 29) * 16)
                                    : (unsigned)(L_FILT + j4 * 16640 + ((lane >> 1) & 3) * 4160 + 4096 + (lane & 1) * 16);
      *(LAS v4u*)(L + za) = v4u{0u, 0u, 0u, 0u};
    }
    W2_RANGE(84, 112);
    WAITV(56);
    TIE(kvt[0]); TIE(kvt[1]);
    *(LAS v4u*)(L + L_U + 480 + j4 * 1024 + vl16) = kvt[0];
    *(LAS v4u*)(L + L_U + 4608 + 480 + j4 * 1024 + vl16) = kvt[1];
    STAMP(12);
  }
  BAR();
  STAMP(13);
  if (CW) {
    W2_RANGE(7 * W2S, 7 * W2S + W2D);
    if (g1) {
      WAITV(W2D);
      if (COUNT_LAST(1, 3u) && (threadIdx.x & 63) == 0) {
        const unsigned fo = (unsigned)(WS_FH + (R * 16 + c) * FSTR);
        FLAG_ST(fo, tagv, ws);
      }
    }
    STAMP(27);
    W2_RANGE(7 * W2S + W2D, 112);
    if (!g1 && id == 8 * 28 && w == 0) {
      FRESH_IDS();
#pragma unroll 1
      for (int part = 0; part < 4; ++part) {
        const unsigned eo = (unsigned)(WS_F0 + (part * 64 + lane) * FSTR);
        spin_flags(v2u{~tlo, 0u}, eo, ws, tlo, thi);
      }
      if (lane == 0) { const unsigned z = 0; FLAG_ST(z, tagv, ws); }
    }
  } else {
    FRESH_IDS();
    const int ch = j4 >> 1, mh = j4 & 1;
    f32x4 ca[8];
#pragma unroll
    for (int i = 0; i < 8; ++i) ca[i] = f32x4{0.f, 0.f, 0.f, 0.f};
    unsigned abo = (unsigned)(L_FILT + ch * 16640 + (3 - (li & 3)) * 4160 + 2 * (2028 + 8 * g - 4 * (li >> 2)) - 512 * 7 - 256 * mh - 192);
    unsigned abo2 = abo + 8;
    TIE(abo2);
    LAS char* const bb = L + L_U + ch * 4608 + 2 * (224 + 16 * li + 8 * g) - 256 * mh - 192;
#define CV_A0(mm, i) Aa[mm][i] = *(LAS v2u*)(L + abo + (3 - (mm)) * 64 + 512 * (7 - (i)))
#define CV_A1(mm, i) Ab[mm][i] = *(LAS v2u*)(L + abo2 + (3 - (mm)) * 64 + 512 * (7 - (i)))
#define CV_B(mm, j) B[mm][j] = *(LAS v4u*)(bb + (3 - (mm)) * 64 + 512 * (j))
#define CV_MM(mm, i, j) ca[(i) + (j)] = mfma16(v4u{Aa[mm][i].x, Aa[mm][i].y, Ab[mm][i].x, Ab[mm][i].y}, B[mm][j], ca[(i) + (j)])
    v2u Aa[4][8], Ab[4][8];
    v4u B[4][8];
    CV_A0(0, 0); SCHED(); CV_A1(0, 0); SCHED();
#pragma unroll
    for (int j = 0; j < 8; ++j) CV_B(0, j);
    SCHED();
    CV_A0(0, 1); SCHED(); CV_A1(0, 1); SCHED(); CV_A0(0, 2); SCHED(); CV_A1(0, 2); SCHED();
#define CV_STEP(mm) \
    CV_MM(mm, 0, 0); SCHED(); CV_A0(mm, 3); SCHED(); \
    CV_MM(mm, 0, 1); SCHED(); CV_A1(mm, 3); SCHED(); \
    CV_MM(mm, 0, 2); SCHED(); CV_B((mm) + 1, 0); SCHED(); \
    CV_MM(mm, 0, 3); SCHED(); \
    CV_MM(mm, 0, 4); SCHED(); CV_A0(mm, 4); SCHED(); \
    CV_MM(mm, 0, 5); SCHED(); CV_A1(mm, 4); SCHED(); \
    CV_MM(mm, 0, 6); SCHED(); CV_B((mm) + 1, 1); SCHED(); \
    CV_MM(mm, 0, 7); SCHED(); \
    CV_MM(mm, 1, 0); SCHED(); CV_A0(mm, 5); SCHED(); \
    CV_MM(mm, 1, 1); SCHED(); CV_A1(mm, 5); SCHED(); \
    CV_MM(mm, 1, 2); SCHED(); CV_B((mm) + 1, 2); SCHED(); \
    CV_MM(mm, 1, 3); SCHED(); \
    CV_MM(mm, 1, 4); SCHED(); CV_A0(mm, 6); SCHED(); \
    CV_MM(mm, 1, 5); SCHED(); CV_A1(mm, 6); SCHED(); \
    CV_MM(mm, 1, 6); SCHED(); CV_A0(mm, 7); SCHED(); \
    CV_MM(mm, 2, 0); SCHED(); \
    CV_MM(mm, 2, 1); SCHED(); CV_A1(mm, 7); SCHED(); \
    CV_MM(mm, 2, 2); SCHED(); CV_B((mm) + 1, 3); SCHED(); \
    CV_MM(mm, 2, 3); SCHED(); CV_B((mm) + 1, 4); SCHED(); \
    CV_MM(mm, 2, 4); SCHED(); \
    CV_MM(mm, 2, 5); SCHED(); CV_A0((mm) + 1, 0); SCHED(); \
    CV_MM(mm, 3, 0); SCHED(); CV_A1((mm) + 1, 0); SCHED(); \
    CV_MM(mm, 3, 1); SCHED(); CV_B((mm) + 1, 5); SCHED(); \
    CV_MM(mm, 3, 2); SCHED(); \
    CV_MM(mm, 3, 3); SCHED(); CV_B((mm) + 1, 6); SCHED(); \
    CV_MM(mm, 3, 4); SCHED(); CV_B((mm) + 1, 7); SCHED(); \
    CV_MM(mm, 4, 0); SCHED(); CV_A0((mm) + 1, 1); SCHED(); \
    CV_MM(mm, 4, 1); SCHED(); \
    CV_MM(mm, 4, 2); SCHED(); CV_A1((mm) + 1, 1); SCHED(); \
    CV_MM(mm, 4, 3); SCHED(); CV_A0((mm) + 1, 2); SCHED(); \
    CV_MM(mm, 5, 0); SCHED(); CV_A1((mm) + 1, 2); SCHED(); \
    CV_MM(mm, 5, 1); SCHED(); \
    CV_MM(mm, 5, 2); SCHED(); \
    CV_MM(mm, 6, 0); SCHED(); \
    CV_MM(mm, 6, 1); SCHED(); \
    CV_MM(mm, 7, 0); SCHED(); \
    do {} while (0)
    CV_STEP(0); CV_STEP(1); CV_STEP(2);
    LAS char* const rb = L + L_RED + j4 * 8192 + lane * 16;
    CV_MM(3, 0, 0); SCHED(); CV_A0(3, 3); SCHED();
    CV_MM(3, 0, 1); SCHED(); CV_A1(3, 3); SCHED();
    CV_MM(3, 1, 0); SCHED(); CV_A0(3, 4); SCHED();
    CV_MM(3, 0, 2); SCHED(); CV_A1(3, 4); SCHED(); *(LAS f32x4*)(rb) = ca[0]; SCHED();
    CV_MM(3, 1, 1); SCHED(); CV_A0(3, 5); SCHED(); *(LAS f32x4*)(rb + 1 * 1024) = ca[1]; SCHED();
    CV_MM(3, 2, 0); SCHED(); CV_A1(3, 5); SCHED();
    CV_MM(3, 0, 3); SCHED(); CV_A0(3, 6); SCHED();
    CV_MM(3, 1, 2); SCHED(); CV_A1(3, 6); SCHED(); *(LAS f32x4*)(rb + 2 * 1024) = ca[2]; SCHED();
    CV_MM(3, 2, 1); SCHED(); CV_A0(3, 7); SCHED();
    CV_MM(3, 3, 0); SCHED(); CV_A1(3, 7); SCHED();
    CV_MM(3, 0, 4); SCHED();
    CV_MM(3, 1, 3); SCHED(); *(LAS f32x4*)(rb + 3 * 1024) = ca[3]; SCHED();
    CV_MM(3, 2, 2); SCHED();
    CV_MM(3, 3, 1); SCHED();
    CV_MM(3, 4, 0); SCHED();
    CV_MM(3, 0, 5); SCHED();
    CV_MM(3, 1, 4); SCHED(); *(LAS f32x4*)(rb + 4 * 1024) = ca[4]; SCHED();
    CV_MM(3, 2, 3); SCHED();
    CV_MM(3, 3, 2); SCHED();
    CV_MM(3, 4, 1); SCHED();
    CV_MM(3, 5, 0); SCHED();
    CV_MM(3, 0, 6); SCHED();
    CV_MM(3, 1, 5); SCHED(); *(LAS f32x4*)(rb + 5 * 1024) = ca[5]; SCHED();
    CV_MM(3, 2, 4); SCHED();
    CV_MM(3, 3, 3); SCHED();
    CV_MM(3, 4, 2); SCHED();
    CV_MM(3, 5, 1); SCHED();
    CV_MM(3, 6, 0); SCHED();
    CV_MM(3, 0, 7); SCHED();
    CV_MM(3, 1, 6); SCHED(); *(LAS f32x4*)(rb + 6 * 1024) = ca[6]; SCHED();
    CV_MM(3, 2, 5); SCHED();
    CV_MM(3, 3, 4); SCHED();
    CV_MM(3, 4, 3); SCHED();
    CV_MM(3, 5, 2); SCHED();
    CV_MM(3, 6, 1); SCHED();
    CV_MM(3, 7, 0); SCHED();
    *(LAS f32x4*)(rb + 7 * 1024) = ca[7];
  }
  v2u hfl;
  unsigned ph;
  {
    FRESH_IDS();
    const int c_lo = (7 * w) >> 2, c_hi = (7 * w + 6) >> 2;
    int cc = c_lo + (lane & 3);
    cc = cc > c_hi ? c_hi : cc;
    ph = (unsigned)(WS_FH + ((st >> 1) * 16 + cc) * FSTR);
    GLD2_SC1(hfl, ph, ws, 0);
  }
  STAMP(14);
  BAR();
  STAMP(15);
  {
    FRESH_IDS();
    const f32x4 c0 = *(LAS f32x4*)(L + L_RED + w * 1024 + lane * 16) + *(LAS f32x4*)(L + L_RED + 8192 + w * 1024 + lane * 16);
    const f32x4 c1 = *(LAS f32x4*)(L + L_RED + 16384 + w * 1024 + lane * 16) + *(LAS f32x4*)(L + L_RED + 24576 + w * 1024 + lane * 16);
    const unsigned so = (unsigned)(256 * w + 16 * li + 4 * g);
    const v2u k0 = *(LAS v2u*)(L + L_U + 480 + so * 2), k1 = *(LAS v2u*)(L + L_U + 4608 + 480 + so * 2);
    const float D0 = blo(dd), D1 = bhi(dd);
    const v4u yv = v4u{pk_bf16(c0[0] + blo(k0.x) * D0, c1[0] + blo(k1.x) * D1), pk_bf16(c0[1] + bhi(k0.x) * D0, c1[1] + bhi(k1.x) * D1),
                       pk_bf16(c0[2] + blo(k0.y) * D0, c1[2] + blo(k1.y) * D1), pk_bf16(c0[3] + bhi(k0.y) * D0, c1[3] + bhi(k1.y) * D1)};
    const unsigned yo = so * 4;
    GST4_SC1(yo, yv, ws + WS_Y2 + (size_t)dpair * 8192, 0);
    WAITV(1);
    TIE(hfl);
  }
  STAMP(16);
#define COUNT_Y()                                                                   \
  do {                                                                              \
    if (COUNT_LAST(0, 7u) && (threadIdx.x & 63) == 0) {                             \
      const unsigned fo = (unsigned)(WS_FY + (n * 32 + st) * FSTR);                 \
      FLAG_ST(fo, tagv, ws);                                                        \
    }                                                                               \
  } while (0)
  const bool slow = !all_eq(hfl, tlo, thi);
  if (slow) {
    asm volatile("global_load_dwordx2 %0, %1, %2 sc1 ; SPIN\n\ts_waitcnt vmcnt(0) ; SPIN" : "=&v"(hfl) : "v"(ph), "s"(ws) : "memory");
    COUNT_Y();
    spin_flags(hfl, ph, ws, tlo, thi);
  }
  STAMP(17);
  v4u hf[HWIN];
  const char* const hb = ws + WS_H + (size_t)(4 * st * 56 + 7 * w) * 1024 + 3072;
#define H_LOAD(x) GLD4_SC1(hf[(x) % HWIN], vl16, hb + ((((w >> 1) + 1 + (x) / 7) & 3) * 57344), ((x) % 7) * 1024 - 3072)
#pragma unroll
  for (int x = 0; x < HWIN; ++x) H_LOAD(x);
  STAMP(18);
  v4u w2f[28];
#pragma unroll
  for (int m_ = 0; m_ < 112; ++m_) TIE(w2r[m_]);
#pragma unroll
  for (int kl = 0; kl < 7; ++kl) {
#pragma unroll
    for (int hf_ = 0; hf_ < 2; ++hf_)
#pragma unroll
      for (int e2 = 0; e2 < 4; ++e2) {
        const unsigned lo_ = w2r[kl * 16 + 4 * e2 + hf_], hi_ = w2r[kl * 16 + 4 * e2 + 2 + hf_];
        w2f[kl * 4 + 2 * hf_][e2] = __builtin_amdgcn_perm(hi_, lo_, 0x05040100u);
        w2f[kl * 4 + 2 * hf_ + 1][e2] = __builtin_amdgcn_perm(hi_, lo_, 0x07060302u);
      }
#pragma unroll
    for (int j = 0; j < 4; ++j) TIE(w2f[kl * 4 + j]);
  }
  STAMP(28);
  unsigned pw, pr;
  {
    FRESH_IDS();
    pw = (unsigned)(w * 16384 + g * 256 + ((li ^ (g << 2)) * 16));
    const int r8 = lane >> 3, c8 = lane & 7, gg = c8 & 3;
    pr = (unsigned)((w >> 1) * 4096 + (c8 >> 2) * 2048 + gg * 256 + (((8 * (w & 1) + r8) ^ (gg << 2)) * 16));
  }
  v2u yf;
  unsigned py;
  v4u yt, qr;
  float gv;
  f32x4 a2[4], b0, b1;
#pragma unroll
  for (int x = 0; x < 28; ++x) {
    WAITV(g2_wait(x));
    TIE(hf[x % HWIN]);
    if (x == 0 && !slow) COUNT_Y();
    if (x == 0) STAMP(19);
    f32x4 r0, r1;
    if (x >= 21 && x < 27) {
      LAS char* const p_ = L + pr + ((((w & 6) + 2 + (x - 21)) & 7) * 16384);
      r0 = *(LAS f32x4*)(p_);
      r1 = *(LAS f32x4*)(p_ + 1024);
    }
    if (x % 7 == 0) {
#pragma unroll
      for (int j = 0; j < 4; ++j) a2[j] = f32x4{0.f, 0.f, 0.f, 0.f};
    }
    SCHED();
#pragma unroll
    for (int j = 0; j < 4; ++j) a2[j] = mfma16(w2f[(x % 7) * 4 + j], hf[x % HWIN], a2[j]);
    SCHED();
    if (x == 21) { b0 = r0; b1 = r1; }
    if (x > 21 && x < 27) { b0 += r0; b1 += r1; TIE(b0); TIE(b1); }
    if (x + HWIN < 28) H_LOAD(x + HWIN);
    if (x == XPOLL) {
      FRESH_IDS();
      py = lane < 32 ? (unsigned)(WS_FY + (n * 32 + 4 * w + (lane & 3)) * FSTR) : (unsigned)(WS_F0 + ((lane & 7) * 32 + st) * FSTR);
      GLD2_SC1(yf, py, ws, 0);
    }
    if (x == XCHK) {
      FRESH_IDS();
#pragma unroll
      for (int j = 0; j < 4; ++j) TIE(a2[j]);
      WAITV(g2_wait_poll());
      TIE(yf);
      STAMP(20);
      spin_flags(yf, py, ws, tlo, thi);
      STAMP(21);
      const unsigned yo = (unsigned)(g * 8192 + li * 16);
      GLD4_SC1(yt, yo, ws + WS_Y2 + (size_t)(32 * n + 4 * w) * 8192 + st * 256, 0);
      const unsigned go = (unsigned)((lane & 7) * 256 + (lane >> 3) * 4);
      GLD1_SC1(gv, go, ws + WS_GP + st * 2048 + w * 32, 0);
      const unsigned qo = (unsigned)((lane >> 3) * 1024 + (lane & 7) * 16);
      GLD4(qr, qo, q + (size_t)(64 * st + 8 * w) * 1024 + n * 128, 0);
    }
    if (x == 5) BAR();
    if (x % 7 == 6) {
      LAS char* const pb_ = L + pw + ((((w >> 1) + 1 + x / 7) & 3) * 4096);
#pragma unroll
      for (int j = 0; j < 4; ++j) *(LAS f32x4*)(pb_ + j * 1024) = a2[j];
    }
    if (x == 20) { BAR(); STAMP(22); }
    SCHED();
  }
  STAMP(23);
  float gt;
  {
    FRESH_IDS();
    WAITV(0);
    TIE(yt); TIE(gv); TIE(qr);
    *(LAS v4u*)(L + L_YT + (4 * w + g) * 256 + w * 16 + li * 16) = yt;
    gt = sigmoidf_(sum8(gv));
  }
  STAMP(24);
  BAR();
  STAMP(25);
  {
    FRESH_IDS();
    const int r8 = lane >> 3, c8 = lane & 7;
    LAS char* const p_ = L + pr + (w & 6) * 16384;
    b0 += *(LAS f32x4*)(p_) + *(LAS f32x4*)(p_ + 16384);
    b1 += *(LAS f32x4*)(p_ + 1024) + *(LAS f32x4*)(p_ + 16384 + 1024);
    unsigned yv[4];
#pragma unroll
    for (int i = 0; i < 4; ++i) yv[i] = *(LAS unsigned*)(L + L_YT + c8 * 1040 + i * 256 + (8 * w + r8) * 4);
    const float og = 1.0f - gt;
    v4u o;
#pragma unroll
    for (int i = 0; i < 4; ++i)
      o[i] = pk_bf16((gt * blo(qr[i])) * blo(yv[i]) + og * b0[i], (gt * bhi(qr[i])) * bhi(yv[i]) + og * b1[i]);
    const unsigned oo = (unsigned)(r8 * 1024 + c8 * 16);
    asm volatile(".Lpf_end%0:" ::"i"((g1 ? 2 : 0) + ROLE));
    GST4(oo, o, out + (size_t)(64 * st + 8 * w) * 1024 + n * 128, 0);
  }
}

#define CODE_PF 0

extern "C" NOPK __global__ __launch_bounds__(512, 2) void fused_kernel(const char* __restrict__ q, const char* __restrict__ k, const char* __restrict__ v,
                                                       const char* __restrict__ sk, const char* __restrict__ ssmk, const char* __restrict__ ssmD,
                                                       const char* __restrict__ w1, const char* __restrict__ w2, const char* __restrict__ gw,
                                                       char* out, char* ws) {
#define TE
  unsigned cpf = 0;
  const int w = __builtin_amdgcn_readfirstlane(threadIdx.x >> 6);
  if (__builtin_expect((blockIdx.x >> 3) < 28, 1)) {
    if (w < 4) body<true, 0>(q, k, v, sk, ssmk, ssmD, w1, w2, gw, out, ws, w, cpf TE);
    else body<true, 1>(q, k, v, sk, ssmk, ssmD, w1, w2, gw, out, ws, w, cpf TE);
  } else {
    if (w < 4) body<false, 0>(q, k, v, sk, ssmk, ssmD, w1, w2, gw, out, ws, w, cpf TE);
    else body<false, 1>(q, k, v, sk, ssmk, ssmD, w1, w2, gw, out, ws, w, cpf TE);
  }
}

extern "C" void kernel_launch(void* const* d_in, const int* in_sizes, int n_in, void* d_out, int out_size, void* d_ws, size_t ws_size,
                              hipStream_t stream) {
  (void)in_sizes; (void)n_in; (void)out_size; (void)ws_size;
  (void)hipFuncSetAttribute((const void*)fused_kernel, hipFuncAttributeMaxDynamicSharedMemorySize, LDS_BYTES);
  fused_kernel<<<256, 512, LDS_BYTES, stream>>>((const char*)d_in[0], (const char*)d_in[1], (const char*)d_in[2], (const char*)d_in[3],
                                                (const char*)d_in[4], (const char*)d_in[5], (const char*)d_in[7], (const char*)d_in[8],
                                                (const char*)d_in[9], (char*)d_out, (char*)d_ws);
}
```

```cpp
#include <hip/hip_runtime.h>
#include <stdint.h>

typedef unsigned v4u __attribute__((ext_vector_type(4)));
typedef unsigned v2u __attribute__((ext_vector_type(2)));
typedef float f32x4 __attribute__((ext_vector_type(4)));
typedef float f32x2 __attribute__((ext_vector_type(2)));
typedef short bf16x8 __attribute__((ext_vector_type(8)));
typedef short s16x4 __attribute__((ext_vector_type(4)));
typedef __bf16 bf2 __attribute__((ext_vector_type(2)));
typedef unsigned long long u64;
#define LAS __attribute__((address_space(3)))
#if defined(__HIP_DEVICE_COMPILE__) && !defined(ALLOW_PK)
#define NOPK __attribute__((target("no-packed-fp32-ops")))
#else
#define NOPK
#endif

#define CODE_PF 0
#define KSTAMP 0
#define SLEEP 4

#define FSTR 128
constexpr unsigned WS_F0 = 786432;
constexpr unsigned WS_FH = 851968;
constexpr unsigned WS_FY = 917504;
constexpr unsigned WS_GP = 16384;
constexpr unsigned WS_TS = 262144;
constexpr unsigned WS_KVT = 1u << 20;
constexpr unsigned WS_Y2 = 3u << 20;
constexpr unsigned WS_H = 5u << 20;

constexpr unsigned L_KB = 131072;
constexpr unsigned L_VB = 140288;
constexpr unsigned L_YT = 131072;
constexpr unsigned L_CNT = 148480;
constexpr unsigned L_FILT = 0;
constexpr unsigned L_U = 33280;
constexpr unsigned L_RED = 65536;
constexpr unsigned LDS_BYTES = 148544;

constexpr int SPIN_CAP = 1 << 16;

NOPK __device__ __forceinline__ unsigned pk_bf16(float a, float b) {
  f32x2 v = {a, b};
  return __builtin_bit_cast(unsigned, __builtin_convertvector(v, bf2));
}
NOPK __device__ __forceinline__ float blo(unsigned u) { return __builtin_bit_cast(float, u << 16); }
NOPK __device__ __forceinline__ float bhi(unsigned u) { return __builtin_bit_cast(float, u & 0xffff0000u); }
NOPK __device__ __forceinline__ f32x4 mfma16(v4u a, v4u b, f32x4 c) {
  return __builtin_amdgcn_mfma_f32_16x16x32_bf16(__builtin_bit_cast(bf16x8, a), __builtin_bit_cast(bf16x8, b), c, 0, 0, 0);
}
NOPK __device__ __forceinline__ float sigmoidf_(float x) {
  return __builtin_amdgcn_rcpf(1.0f + __builtin_amdgcn_exp2f(-1.4426950408889634f * x));
}
NOPK __device__ __forceinline__ float silu1(float x) { return x * sigmoidf_(x); }
NOPK __device__ __forceinline__ float sum8(float x) {
  x += __builtin_bit_cast(float, __builtin_amdgcn_update_dpp(0, __builtin_bit_cast(int, x), 0xB1, 0xf, 0xf, true));
  x += __builtin_bit_cast(float, __builtin_amdgcn_update_dpp(0, __builtin_bit_cast(int, x), 0x4E, 0xf, 0xf, true));
  x += __builtin_bit_cast(float, __builtin_amdgcn_update_dpp(0, __builtin_bit_cast(int, x), 0x141, 0xf, 0xf, true));
  return x;
}

#define GLD1(dst, voff, sbase, imm) asm volatile("global_load_dword %0, %1, %2 offset:%3" : "=v"(dst) : "v"(voff), "s"(sbase), "i"(imm))
#define GLD1_SC1(dst, voff, sbase, imm) asm volatile("global_load_dword %0, %1, %2 offset:%3 sc1" : "=v"(dst) : "v"(voff), "s"(sbase), "i"(imm))
#define GLD2(dst, voff, sbase, imm) asm volatile("global_load_dwordx2 %0, %1, %2 offset:%3" : "=v"(dst) : "v"(voff), "s"(sbase), "i"(imm))
#define GLD2_SC1(dst, voff, sbase, imm) asm volatile("global_load_dwordx2 %0, %1, %2 offset:%3 sc1" : "=v"(dst) : "v"(voff), "s"(sbase), "i"(imm))
#define GLD4(dst, voff, sbase, imm) asm volatile("global_load_dwordx4 %0, %1, %2 offset:%3" : "=v"(dst) : "v"(voff), "s"(sbase), "i"(imm))
#define GLD4_SC1(dst, voff, sbase, imm) asm volatile("global_load_dwordx4 %0, %1, %2 offset:%3 sc1" : "=v"(dst) : "v"(voff), "s"(sbase), "i"(imm))
#define GST4_SC1(voff, data, sbase, imm) asm volatile("global_store_dwordx4 %0, %1, %2 offset:%3 sc1\n\ts_nop 1" ::"v"(voff), "v"(data), "s"(sbase), "i"(imm) : "memory")
#define GST4(voff, data, sbase, imm) asm volatile("global_store_dwordx4 %0, %1, %2 offset:%3 nt\n\ts_nop 1" ::"v"(voff), "v"(data), "s"(sbase), "i"(imm) : "memory")
#define FLAG_ST(voff, data, sbase) asm volatile("global_store_dwordx2 %0, %1, %2 sc1 ; FLAG" ::"v"(voff), "v"(data), "s"(sbase) : "memory")
#define GST1_SC1(voff, data, sbase, imm) asm volatile("global_store_dword %0, %1, %2 offset:%3 sc1" ::"v"(voff), "v"(data), "s"(sbase), "i"(imm) : "memory")
#define DMA16(ldsaddr, voff, sbase) asm volatile("s_mov_b32 m0, %0\n\ts_nop 0\n\tglobal_load_lds_dwordx4 %1, %2" ::"s"(ldsaddr), "v"(voff), "s"(sbase) : "memory")
#define WAITV(n) asm volatile("s_waitcnt vmcnt(%0)" ::"i"(n) : "memory")
#define BAR() asm volatile("s_waitcnt lgkmcnt(0)\n\ts_barrier" ::: "memory")
#define TIE(x) asm volatile("" : "+v"(x))
#define SCHED() __builtin_amdgcn_sched_barrier(0)
#define FRESH_IDS()                                         \
  int tid = threadIdx.x;                                    \
  asm volatile("" : "+v"(tid));                             \
  const int lane = tid & 63, li = lane & 15, g = lane >> 4; \
  (void)li; (void)g;

#define STAMP(i) do {} while (0)

#define NOSYNC 0
NOPK __device__ __forceinline__ bool all_eq(v2u f, unsigned tlo, unsigned thi) {
  if (NOSYNC) return true;
  return __builtin_amdgcn_ballot_w64(f.x == tlo && f.y == thi) == ~0ull;
}
NOPK __device__ __forceinline__ void spin_flags(v2u f, unsigned voff, const char* ws, unsigned tlo, unsigned thi) {
  int spins = 0;
  while (__builtin_expect(!all_eq(f, tlo, thi), 0)) {
    if (++spins > SPIN_CAP) break;
    __builtin_amdgcn_s_sleep(SLEEP);
    asm volatile("global_load_dwordx2 %0, %1, %2 sc1 ; SPIN\n\ts_waitcnt vmcnt(0) ; SPIN" : "=&v"(f) : "v"(voff), "s"(ws) : "memory");
  }
}

#define HWIN 8
#define XPOLL 14
#define XCHK 20
#define DPRE_A 12
#define F0_IT 2
#define W2S 4
#define W2D 32
NOPK __host__ __device__ __forceinline__ constexpr int g2_issued(int x) {
  int c = HWIN;
  for (int t = 0; t < x; ++t) c += (t + HWIN < 28 ? 1 : 0) + (t == XPOLL ? 1 : 0) + (t == XCHK ? 3 : 0);
  return c;
}
NOPK __host__ __device__ __forceinline__ constexpr int g2_pos_h(int j) { return j < HWIN ? j : g2_issued(j - HWIN); }
NOPK __host__ __device__ __forceinline__ constexpr int g2_wait(int x) { return g2_issued(x) - g2_pos_h(x) - 1; }
NOPK __host__ __device__ __forceinline__ constexpr int g2_wait_poll() { return (g2_issued(XCHK) + (XCHK + HWIN < 28 ? 1 : 0)) - (g2_issued(XPOLL) + (XPOLL + HWIN < 28 ? 1 : 0)) - 1; }
template <bool g1, int ROLE>
NOPK __device__ __forceinline__ void body(const char* __restrict__ q, const char* __restrict__ k, const char* __restrict__ v,
                                     const char* __restrict__ sk, const char* __restrict__ ssmk, const char* __restrict__ ssmD,
                                     const char* __restrict__ w1, const char* __restrict__ w2, const char* __restrict__ gw,
                                     char* out, char* ws, const int w, unsigned cpf
) {
  extern __shared__ __attribute__((aligned(16))) char smem_[];
  LAS char* const L = (LAS char*)smem_;
  const unsigned lbase = (unsigned)(uintptr_t)L;
  constexpr bool CW = ROLE == 0, HW = ROLE == 1;
  const int id = blockIdx.x, xcd = id & 7, loc = id >> 3;
  const int st = 4 * xcd + (loc >> 3), n = loc & 7;
  const int R = 4 * (xcd >> 1) + (g1 ? loc / 7 : 0), c = 7 * (xcd & 1) + loc % 7;
  const int dpair = 32 * n + st;
  const int j4 = w & 3;
#define BLKROT (n + 2 * (loc >> 3))
  const int blk = (w + BLKROT) & 7;

  if (HW) __builtin_amdgcn_s_setprio(3);
  u64 pfpc = 0; unsigned pfo = 0; (void)pfpc; (void)pfo;
  constexpr bool G1C = g1 && CW;
  int st8 = st * 8;
  asm volatile("" : "+s"(st8));
  unsigned vx0 = 0, vx1 = 0, vw0 = 0, vw1 = 0;
  const char* const xb = q + (size_t)R * 131072 + j4 * 32768;
  const char* const wb = w1 + c * 256 + j4 * 57344;
  if (G1C) {
    FRESH_IDS();
    vx0 = (unsigned)((lane >> 3) * 1024 + (((lane & 7) ^ g) * 16));
    vx1 = (unsigned)((8 + (lane >> 3)) * 1024 + (((lane & 7) ^ (4 + g)) * 16));
    vw0 = (unsigned)(g * 3584 + (((li >> 1) ^ g) * 32) + (li & 1) * 16);
    vw1 = (unsigned)((8 + g) * 3584 + (((li >> 1) ^ (g | 4)) * 32) + (li & 1) * 16);
  }
#define ROTK 0
  const int rot = ROTK;
#define DMA_PIECE(ch, p)                                                                                      \
  do {                                                                                                        \
    if (G1C) {                                                                                                \
      const unsigned sl_ = lbase + ((ch) & 3) * 32768 + j4 * 4096;                                            \
      const int kc_ = ((ch) + rot) & 7;                                                                       \
      if ((p) == 0) DMA16(sl_, vx0, xb + kc_ * 128);                                                          \
      if ((p) == 1) DMA16(sl_ + 16384, vw0, wb + kc_ * 229376);                                               \
      if ((p) == 2) DMA16(sl_ + 1024, vx1, xb + kc_ * 128);                                                   \
      if ((p) == 3) DMA16(sl_ + 16384 + 1024, vw0, wb + kc_ * 229376 + 14336);                                \
      if ((p) == 4) DMA16(sl_ + 2048, vx0, xb + kc_ * 128 + 16384);                                           \
      if ((p) == 5) DMA16(sl_ + 16384 + 2048, vw1, wb + kc_ * 229376);                                        \
      if ((p) == 6) DMA16(sl_ + 3072, vx1, xb + kc_ * 128 + 16384);                                           \
      if ((p) == 7) DMA16(sl_ + 16384 + 3072, vw1, wb + kc_ * 229376 + 14336);                                \
    }                                                                                                         \
  } while (0)
#define DMA_RANGE(lo, hi) _Pragma("unroll") for (int d_ = (lo); d_ < (hi); ++d_) DMA_PIECE(d_ >> 3, d_ & 7)
  DMA_RANGE(0, 8);
  unsigned cpf2 = 0;
  DMA_RANGE(8, DPRE_A);

  u64 epoch;
  unsigned dd;
  asm volatile("s_load_dwordx2 %0, %1, 0x0" : "=s"(epoch) : "s"(ws));
  asm volatile("s_load_dword %0, %1, 0x0" : "=s"(dd) : "s"(ssmD + dpair * 4));

  unsigned vl16;
  { FRESH_IDS(); vl16 = (unsigned)(lane * 16); }
  v4u fhi[2], skr, qa, qb, gwr;
  v2u flo[2];
  if (HW) {
    FRESH_IDS();
    const unsigned ro = (unsigned)((lane >> 3) * 1024);
    const char* const kb_ = k + (size_t)st * 65536 + n * 128 + j4 * 16384;
    const char* const vb_ = v + (size_t)st * 65536 + n * 128 + j4 * 16384;
    const unsigned ka = ro + (((lane & 7) ^ ((2 * j4 + 1) & 7)) * 16), kc = ro + (((lane & 7) ^ ((2 * j4 + 2) & 7)) * 16);
    const unsigned va = ro + (((lane & 7) ^ (2 * j4)) * 16);
    const unsigned qo = ro + (lane & 7) * 16;
    DMA16(lbase + L_KB + 1024 + j4 * 2048, ka, kb_);
    DMA16(lbase + L_KB + 2048 + j4 * 2048, kc, kb_ + 8192);
    DMA16(lbase + L_VB + j4 * 2048, va, vb_);
    DMA16(lbase + L_VB + 1024 + j4 * 2048, ka, vb_ + 8192);
    if (j4 == 0) DMA16(lbase + L_KB, qo, kb_ - (st > 0 ? 8192 : 0));
    const unsigned so = (unsigned)((lane >> 3) * 16);
    GLD4(skr, so, sk + n * 512 + j4 * 128, 0);
    const char* const qb_ = q + (size_t)st * 65536 + n * 128 + j4 * 16384;
    GLD4(qa, qo, qb_, 0);
    GLD4(qb, qo, qb_ + 8192, 0);
    const unsigned go = (unsigned)((lane & 7) * 16);
    GLD4(gwr, go, gw + n * 128, 0);
    if (tid == 256) *(LAS v4u*)(L + L_CNT) = v4u{0u, 0u, 0u, 0u};
    WAITV(3);
  }
  if (CW) STAMP(1);
  BAR();
  STAMP(2);
  asm volatile("" : "+s"(epoch), "+s"(dd));
  const u64 tag = epoch * 6364136223846793005ull + 1442695040888963407ull;
  const unsigned tlo = (unsigned)tag, thi = (unsigned)(tag >> 32);
  const v2u tagv = v2u{tlo, thi};
  DMA_RANGE(DPRE_A, 24);
  if (G1C) WAITV(16 + (CODE_PF ? 2 : 0));
#define KV_HALF(SEL, ta, tb, off)                                                                                          \
  do {                                                                                                                    \
    float kf[11], r_[8];                                                                                                  \
    const float t0 = blo(ta), t1 = bhi(ta), t2 = blo(tb), t3 = bhi(tb);                                                   \
    _Pragma("unroll") for (int i = 0; i < 11; ++i) kf[i] = SEL(kk[i]);                                                    \
    _Pragma("unroll") for (int i = 0; i < 8; ++i) r_[i] = (kf[i + 3] * t0 + kf[i + 2] * t1 + kf[i + 1] * t2 + kf[i] * t3) * SEL(vv[i]); \
    const v4u o_ = v4u{pk_bf16(r_[0], r_[1]), pk_bf16(r_[2], r_[3]), pk_bf16(r_[4], r_[5]), pk_bf16(r_[6], r_[7])};        \
    GST4_SC1(ko, o_, ws + WS_KVT + (size_t)n * 262144 + st * 128 + (off), 0);                                             \
  } while (0)
  if (HW) {
    FRESH_IDS();
    TIE(skr);
    const int sg = lane & 7, dp = 8 * j4 + (lane >> 3);
    const unsigned cb = (unsigned)((dp & 3) * 4), ck = (unsigned)(dp >> 2);
    unsigned kk[11], vv[8];
#pragma unroll
    for (int i = 0; i < 3; ++i) kk[i] = *(LAS unsigned*)(L + L_KB + (8 * sg + 5 + i) * 128 + ((ck ^ sg) * 16) + cb);
#pragma unroll
    for (int i = 0; i < 8; ++i) kk[3 + i] = *(LAS unsigned*)(L + L_KB + (8 * sg + 8 + i) * 128 + ((ck ^ ((sg + 1) & 7)) * 16) + cb);
#pragma unroll
    for (int i = 0; i < 8; ++i) vv[i] = *(LAS unsigned*)(L + L_VB + (8 * sg + i) * 128 + ((ck ^ sg) * 16) + cb);
    if (st == 0 && sg == 0) { kk[0] = 0u; kk[1] = 0u; kk[2] = 0u; }
    const unsigned ko = (unsigned)(dp * 8192 + sg * 16);
    KV_HALF(blo, skr.x, skr.y, 0);
    KV_HALF(bhi, skr.z, skr.w, 4096);
    SCHED();
  }
  if (!g1 && CW) WAITV(0);
  BAR();
  STAMP(3);
  if (HW) {
    FRESH_IDS();
    WAITV(2);
    TIE(qa); TIE(qb); TIE(gwr);
    float ga = 0.f, gb = 0.f;
#pragma unroll
    for (int i = 0; i < 4; ++i) {
      ga += blo(qa[i]) * blo(gwr[i]) + bhi(qa[i]) * bhi(gwr[i]);
      gb += blo(qb[i]) * blo(gwr[i]) + bhi(qb[i]) * bhi(gwr[i]);
    }
    ga = sum8(ga);
    gb = sum8(gb);
    const float gs = (lane & 1) ? gb : ga;
    const unsigned gpo = (unsigned)(((lane >> 3) + (lane & 1) * 8) * 4);
    GST1_SC1(gpo, gs, ws + WS_GP + (st8 + n) * 256 + j4 * 64, 0);
    __builtin_amdgcn_s_setprio(0);
    SCHED();
  }

  f32x4 acc1[4][4];
#pragma unroll
  for (int a = 0; a < 4; ++a)
#pragma unroll
    for (int b = 0; b < 4; ++b) acc1[a][b] = f32x4{0.f, 0.f, 0.f, 0.f};
  const int wsx = w & 1, wfx = (w >> 1) & 1;
  unsigned ax0 = 0, aw0 = 0;
  if (G1C) {
    FRESH_IDS();
    __builtin_amdgcn_s_setprio(1);
    ax0 = (unsigned)((wsx * 64 + li) * 128 + ((g ^ (li >> 1)) * 16));
    const unsigned sg = (unsigned)((li >> 2) | ((g & 1) << 2));
    aw0 = (unsigned)(16384 + (8 * g + (li >> 2)) * 256 + (((4 * wfx) ^ sg) * 32) + (lane & 3) * 8);
  }
  v4u xf[2][4];
  v2u wfa[2][4], wfb[2][4];
#define RD_WA(hs, gq) wfa[(hs) & 1][gq] = __builtin_bit_cast(v2u, __builtin_amdgcn_ds_read_tr16_b64_v4i16((LAS s16x4*)(L + (((hs) >> 1) & 3) * 32768 + (aw0 ^ ((gq) * 32)) + ((hs) & 1) * 8192)))
#define RD_WB(hs, gq) wfb[(hs) & 1][gq] = __builtin_bit_cast(v2u, __builtin_amdgcn_ds_read_tr16_b64_v4i16((LAS s16x4*)(L + (((hs) >> 1) & 3) * 32768 + (aw0 ^ ((gq) * 32)) + ((hs) & 1) * 8192 + 1024)))
#define RD_X(hs, gq) xf[(hs) & 1][gq] = *(LAS v4u*)(L + (((hs) >> 1) & 3) * 32768 + (ax0 ^ (((hs) & 1) * 64)) + (gq) * 2048)
#define MM1(hs, t, gq) acc1[t][gq] = mfma16(v4u{wfa[(hs) & 1][t].x, wfa[(hs) & 1][t].y, wfb[(hs) & 1][t].x, wfb[(hs) & 1][t].y}, xf[(hs) & 1][gq], acc1[t][gq])
#define MM_GROUP(hs, gq) _Pragma("unroll") for (int t_ = 0; t_ < 4; ++t_) MM1(hs, t_, gq)

  unsigned w2r[112], vw2;
  { FRESH_IDS(); vw2 = (unsigned)(g * 4096 + li * 4); }
  const char* const w2b = w2 + (size_t)blk * 229376 + n * 128;
#define W2_LOAD(m) GLD1(w2r[m], vw2, w2b + ((m) >> 3) * 16384, (((m) >> 1) & 3) * 1024 + ((m) & 1) * 64)
#define W2_RANGE(lo, hi) _Pragma("unroll") for (int m_ = (lo); m_ < (hi); ++m_) { if (m_ >= 40 && (m_ & 3) == 0) WAITV(56); W2_LOAD(m_); }

#define HALF_STEP(hs, dlo, dhi)                                                                      \
  do {                                                                                               \
    if (G1C) {                                                                                       \
      _Pragma("unroll") for (int u_ = 0; u_ < 16; ++u_) {                                            \
        MM1(hs, u_ & 3, u_ >> 2);                                                                    \
        SCHED();                                                                                     \
        if (u_ == 0) RD_X((hs) + 1, 0);                                                              \
        else if (u_ < 9) { if (u_ & 1) RD_WA((hs) + 1, (u_ - 1) >> 1); else RD_WB((hs) + 1, (u_ - 1) >> 1); } \
        else if (u_ < 12) RD_X((hs) + 1, u_ - 8);                                                    \
        if ((u_ & 3) == 3) { DMA_RANGE((dlo) + (((dhi) - (dlo)) * (u_ >> 2)) / 4, (dlo) + (((dhi) - (dlo)) * ((u_ >> 2) + 1)) / 4); } \
        SCHED();                                                                                     \
      }                                                                                              \
    }                                                                                                \
  } while (0)
#define PIN_ACC1() do { if (G1C) { _Pragma("unroll") for (int a_ = 0; a_ < 16; ++a_) TIE(acc1[a_ & 3][a_ >> 2]); } } while (0)
  v2u pf;
  unsigned pf0;
  v4u kvt[2];
#define G1_HEAD(it, ncw)                                                                             \
  do {                                                                                               \
    PIN_ACC1();                                                                                      \
    if (G1C) WAITV(ncw);                                                                             \
    BAR();                                                                                           \
    STAMP(4 + (it));                                                                                 \
  } while (0)
#define F0_FLAG()                                                          \
  do {                                                                     \
    if (HW && threadIdx.x == 256) {                                        \
      const unsigned fo = (unsigned)(WS_F0 + (n * 32 + st) * FSTR);        \
      FLAG_ST(fo, tagv, ws);                                               \
    }                                                                      \
  } while (0)
  if (G1C) {
#pragma unroll
    for (int gq = 0; gq < 4; ++gq) { RD_WA(0, gq); RD_WB(0, gq); RD_X(0, gq); }
    SCHED();
  }
  HALF_STEP(0, 24, 32);
  G1_HEAD(0, 16);
  TIE(cpf); TIE(cpf2);
  HALF_STEP(1, 32, 36); HALF_STEP(2, 36, 40);
  if (HW && F0_IT == 1) WAITV(0);
  G1_HEAD(1, 16);
  if (F0_IT == 1) F0_FLAG();
  HALF_STEP(3, 40, 44); HALF_STEP(4, 44, 48);
  if (HW && F0_IT == 2) WAITV(0);
  G1_HEAD(2, 16);
  if (F0_IT == 2) F0_FLAG();
  HALF_STEP(5, 48, 52); HALF_STEP(6, 52, 56);
  G1_HEAD(3, 16);
  if (HW) {
    FRESH_IDS();
    const int bi = tid & 255;
    const unsigned fo = (unsigned)((255 - bi) * 16);
    const unsigned fo2 = bi == 255 ? fo : fo - 8;
    const char* const fb_ = ssmk + (size_t)(2 * dpair) * 4096;
    GLD4(fhi[0], fo, fb_, 0);
    GLD2(flo[0], fo2, fb_, 0);
    GLD4(fhi[1], fo, fb_ + 4096, 0);
    GLD2(flo[1], fo2, fb_ + 4096, 0);
  }
  HALF_STEP(7, 56, 60); HALF_STEP(8, 60, 64);
  G1_HEAD(4, 16);
  HALF_STEP(9, 0, 0); HALF_STEP(10, 0, 0);
  if (HW) { W2_RANGE(0, 28); }
  G1_HEAD(5, 8);
  HALF_STEP(11, 0, 0); HALF_STEP(12, 0, 0);
  if (HW) {
    FRESH_IDS();
    pf0 = (unsigned)(WS_F0 + (n * 32 + 8 * j4 + (lane & 7)) * FSTR);
    GLD2_SC1(pf, pf0, ws, 0);
    W2_RANGE(28, 56);
  }
  G1_HEAD(6, 0);
  HALF_STEP(13, 0, 0); HALF_STEP(14, 0, 0);
  STAMP(11);

#define COUNT_LAST(idx, last) \
  (__builtin_amdgcn_readfirstlane((threadIdx.x & 63) == 0 ? __hip_atomic_fetch_add((LAS unsigned*)(L + L_CNT + 4 * (idx)), 1u, __ATOMIC_RELAXED, __HIP_MEMORY_SCOPE_WORKGROUP) : 0u) == (last))
  if (CW) {
    char* const hb_ = ws + WS_H + (size_t)((8 * R + 4 * wsx) * 56 + 4 * c + 2 * wfx) * 1024;
#define SILU_TILE(i, kp)                                                                                               \
  do {                                                                                                                 \
    if (g1) {                                                                                                          \
    float s_[8];                                                                                                       \
    _Pragma("unroll") for (int t2 = 0; t2 < 2; ++t2)                                                                   \
      _Pragma("unroll") for (int r = 0; r < 4; ++r) s_[t2 * 4 + r] = silu1(acc1[2 * (kp) + t2][i][r]);                 \
    const v4u hv = v4u{pk_bf16(s_[0], s_[1]), pk_bf16(s_[2], s_[3]), pk_bf16(s_[4], s_[5]), pk_bf16(s_[6], s_[7])};    \
    GST4_SC1(vl16, hv, hb_ + (i) * 57344, (kp) * 1024);                                                                \
    }                                                                                                                  \
    SCHED();                                                                                                           \
  } while (0)
    if (g1) { MM_GROUP(15, 0); SCHED(); MM_GROUP(15, 1); SCHED(); }
    SILU_TILE(0, 0); W2_RANGE(0 * W2S, 1 * W2S); SILU_TILE(0, 1); W2_RANGE(1 * W2S, 2 * W2S);
    if (g1) { SCHED(); MM_GROUP(15, 2); SCHED(); }
    SILU_TILE(1, 0); W2_RANGE(2 * W2S, 3 * W2S); SILU_TILE(1, 1); W2_RANGE(3 * W2S, 4 * W2S);
    if (g1) { SCHED(); MM_GROUP(15, 3); SCHED(); }
    SILU_TILE(2, 0); W2_RANGE(4 * W2S, 5 * W2S); SILU_TILE(2, 1); W2_RANGE(5 * W2S, 6 * W2S);
    SCHED();
    SILU_TILE(3, 0); W2_RANGE(6 * W2S, 7 * W2S); SILU_TILE(3, 1);
    __builtin_amdgcn_s_setprio(0);
    STAMP(12);
  } else {
    FRESH_IDS();
    WAITV(28);
    TIE(pf);
    spin_flags(pf, pf0, ws, tlo, thi);
    STAMP(26);
    GLD4_SC1(kvt[0], vl16, ws + WS_KVT + (size_t)(2 * dpair) * 4096 + j4 * 1024, 0);
    GLD4_SC1(kvt[1], vl16, ws + WS_KVT + (size_t)(2 * dpair + 1) * 4096 + j4 * 1024, 0);
    W2_RANGE(56, 84);
    const int bi = tid & 255;
#pragma unroll
    for (int ch = 0; ch < 2; ++ch) {
      TIE(fhi[ch]); TIE(flo[ch]);
      if (bi == 255) flo[ch] = v2u{0u, 0u};
      const unsigned W2_ = flo[ch].x, W3 = flo[ch].y, W4 = fhi[ch].x, W5 = fhi[ch].y, W6 = fhi[ch].z, W7 = fhi[ch].w;
      const unsigned S3 = __builtin_amdgcn_alignbit(W3, W3, 16), S4 = __builtin_amdgcn_alignbit(W4, W4, 16), S5 = __builtin_amdgcn_alignbit(W5, W5, 16),
                     S6 = __builtin_amdgcn_alignbit(W6, W6, 16), S7 = __builtin_amdgcn_alignbit(W7, W7, 16);
      const unsigned T3 = (W3 & 0xffffu) | (W2_ & 0xffff0000u), T4 = (W4 & 0xffffu) | (W3 & 0xffff0000u), T5 = (W5 & 0xffffu) | (W4 & 0xffff0000u),
                     T6 = (W6 & 0xffffu) | (W5 & 0xffff0000u), T7 = (W7 & 0xffffu) | (W6 & 0xffff0000u);
      LAS char* fb = L + L_FILT + ch * 16640 + bi * 16;
      *(LAS v4u*)(fb) = v4u{S7, S6, S5, S4};
      *(LAS v4u*)(fb + 4160) = v4u{T7, T6, T5, T4};
      *(LAS v4u*)(fb + 8320) = v4u{S6, S5, S4, S3};
      *(LAS v4u*)(fb + 12480) = v4u{T6, T5, T4, T3};
    }
    if (j4 < 2) {
      const unsigned za = lane < 32 ? (unsigned)(L_U + j4 * 4608 + (lane < 30 ? lane : 29) * 16)
                                    : (unsigned)(L_FILT + j4 * 16640 + ((lane >> 1) & 3) * 4160 + 4096 + (lane & 1) * 16);
      *(LAS v4u*)(L + za) = v4u{0u, 0u, 0u, 0u};
    }
    W2_RANGE(84, 112);
    WAITV(56);
    TIE(kvt[0]); TIE(kvt[1]);
    *(LAS v4u*)(L + L_U + 480 + j4 * 1024 + vl16) = kvt[0];
    *(LAS v4u*)(L + L_U + 4608 + 480 + j4 * 1024 + vl16) = kvt[1];
    STAMP(12);
  }
  BAR();
  STAMP(13);
  if (CW) {
    W2_RANGE(7 * W2S, 7 * W2S + W2D);
    if (g1) {
      WAITV(W2D);
      if (COUNT_LAST(1, 3u) && (threadIdx.x & 63) == 0) {
        const unsigned fo = (unsigned)(WS_FH + (R * 16 + c) * FSTR);
        FLAG_ST(fo, tagv, ws);
      }
    }
    STAMP(27);
    W2_RANGE(7 * W2S + W2D, 112);
    if (!g1 && id == 8 * 28 && w == 0) {
      FRESH_IDS();
#pragma unroll 1
      for (int part = 0; part < 4; ++part) {
        const unsigned eo = (unsigned)(WS_F0 + (part * 64 + lane) * FSTR);
        spin_flags(v2u{~tlo, 0u}, eo, ws, tlo, thi);
      }
      if (lane == 0) { const unsigned z = 0; FLAG_ST(z, tagv, ws); }
    }
  } else {
    FRESH_IDS();
    const int ch = j4 >> 1, mh = j4 & 1;
    f32x4 ca[8];
#pragma unroll
    for (int i = 0; i < 8; ++i) ca[i] = f32x4{0.f, 0.f, 0.f, 0.f};
    unsigned abo = (unsigned)(L_FILT + ch * 16640 + (3 - (li & 3)) * 4160 + 2 * (2028 + 8 * g - 4 * (li >> 2)) - 512 * 7 - 256 * mh - 192);
    unsigned abo2 = abo + 8;
    TIE(abo2);
    LAS char* const bb = L + L_U + ch * 4608 + 2 * (224 + 16 * li + 8 * g) - 256 * mh - 192;
#define CV_A0(mm, i) Aa[mm][i] = *(LAS v2u*)(L + abo + (3 - (mm)) * 64 + 512 * (7 - (i)))
#define CV_A1(mm, i) Ab[mm][i] = *(LAS v2u*)(L + abo2 + (3 - (mm)) * 64 + 512 * (7 - (i)))
#define CV_B(mm, j) B[mm][j] = *(LAS v4u*)(bb + (3 - (mm)) * 64 + 512 * (j))
#define CV_MM(mm, i, j) ca[(i) + (j)] = mfma16(v4u{Aa[mm][i].x, Aa[mm][i].y, Ab[mm][i].x, Ab[mm][i].y}, B[mm][j], ca[(i) + (j)])
    v2u Aa[4][8], Ab[4][8];
    v4u B[4][8];
    CV_A0(0, 0); SCHED(); CV_A1(0, 0); SCHED();
#pragma unroll
    for (int j = 0; j < 8; ++j) CV_B(0, j);
    SCHED();
    CV_A0(0, 1); SCHED(); CV_A1(0, 1); SCHED(); CV_A0(0, 2); SCHED(); CV_A1(0, 2); SCHED();
#define CV_STEP(mm) \
    CV_MM(mm, 0, 0); SCHED(); CV_A0(mm, 3); SCHED(); \
    CV_MM(mm, 0, 1); SCHED(); CV_A1(mm, 3); SCHED(); \
    CV_MM(mm, 0, 2); SCHED(); CV_B((mm) + 1, 0); SCHED(); \
    CV_MM(mm, 0, 3); SCHED(); \
    CV_MM(mm, 0, 4); SCHED(); CV_A0(mm, 4); SCHED(); \
    CV_MM(mm, 0, 5); SCHED(); CV_A1(mm, 4); SCHED(); \
    CV_MM(mm, 0, 6); SCHED(); CV_B((mm) + 1, 1); SCHED(); \
    CV_MM(mm, 0, 7); SCHED(); \
    CV_MM(mm, 1, 0); SCHED(); CV_A0(mm, 5); SCHED(); \
    CV_MM(mm, 1, 1); SCHED(); CV_A1(mm, 5); SCHED(); \
    CV_MM(mm, 1, 2); SCHED(); CV_B((mm) + 1, 2); SCHED(); \
    CV_MM(mm, 1, 3); SCHED(); \
    CV_MM(mm, 1, 4); SCHED(); CV_A0(mm, 6); SCHED(); \
    CV_MM(mm, 1, 5); SCHED(); CV_A1(mm, 6); SCHED(); \
    CV_MM(mm, 1, 6); SCHED(); CV_A0(mm, 7); SCHED(); \
    CV_MM(mm, 2, 0); SCHED(); \
    CV_MM(mm, 2, 1); SCHED(); CV_A1(mm, 7); SCHED(); \
    CV_MM(mm, 2, 2); SCHED(); CV_B((mm) + 1, 3); SCHED(); \
    CV_MM(mm, 2, 3); SCHED(); CV_B((mm) + 1, 4); SCHED(); \
    CV_MM(mm, 2, 4); SCHED(); \
    CV_MM(mm, 2, 5); SCHED(); CV_A0((mm) + 1, 0); SCHED(); \
    CV_MM(mm, 3, 0); SCHED(); CV_A1((mm) + 1, 0); SCHED(); \
    CV_MM(mm, 3, 1); SCHED(); CV_B((mm) + 1, 5); SCHED(); \
    CV_MM(mm, 3, 2); SCHED(); \
    CV_MM(mm, 3, 3); SCHED(); CV_B((mm) + 1, 6); SCHED(); \
    CV_MM(mm, 3, 4); SCHED(); CV_B((mm) + 1, 7); SCHED(); \
    CV_MM(mm, 4, 0); SCHED(); CV_A0((mm) + 1, 1); SCHED(); \
    CV_MM(mm, 4, 1); SCHED(); \
    CV_MM(mm, 4, 2); SCHED(); CV_A1((mm) + 1, 1); SCHED(); \
    CV_MM(mm, 4, 3); SCHED(); CV_A0((mm) + 1, 2); SCHED(); \
    CV_MM(mm, 5, 0); SCHED(); CV_A1((mm) + 1, 2); SCHED(); \
    CV_MM(mm, 5, 1); SCHED(); \
    CV_MM(mm, 5, 2); SCHED(); \
    CV_MM(mm, 6, 0); SCHED(); \
    CV_MM(mm, 6, 1); SCHED(); \
    CV_MM(mm, 7, 0); SCHED(); \
    do {} while (0)
    CV_STEP(0); CV_STEP(1); CV_STEP(2);
    LAS char* const rb = L + L_RED + j4 * 8192 + g * 256 + ((li ^ (g << 2)) * 16);
    CV_MM(3, 0, 0); SCHED(); CV_A0(3, 3); SCHED();
    CV_MM(3, 0, 1); SCHED(); CV_A1(3, 3); SCHED();
    CV_MM(3, 1, 0); SCHED(); CV_A0(3, 4); SCHED();
    CV_MM(3, 0, 2); SCHED(); CV_A1(3, 4); SCHED(); *(LAS f32x4*)(rb) = ca[0]; SCHED();
    CV_MM(3, 1, 1); SCHED(); CV_A0(3, 5); SCHED(); *(LAS f32x4*)(rb + 1 * 1024) = ca[1]; SCHED();
    CV_MM(3, 2, 0); SCHED(); CV_A1(3, 5); SCHED();
    CV_MM(3, 0, 3); SCHED(); CV_A0(3, 6); SCHED();
    CV_MM(3, 1, 2); SCHED(); CV_A1(3, 6); SCHED(); *(LAS f32x4*)(rb + 2 * 1024) = ca[2]; SCHED();
    CV_MM(3, 2, 1); SCHED(); CV_A0(3, 7); SCHED();
    CV_MM(3, 3, 0); SCHED(); CV_A1(3, 7); SCHED();
    CV_MM(3, 0, 4); SCHED();
    CV_MM(3, 1, 3); SCHED(); *(LAS f32x4*)(rb + 3 * 1024) = ca[3]; SCHED();
    CV_MM(3, 2, 2); SCHED();
    CV_MM(3, 3, 1); SCHED();
    CV_MM(3, 4, 0); SCHED();
    CV_MM(3, 0, 5); SCHED();
    CV_MM(3, 1, 4); SCHED(); *(LAS f32x4*)(rb + 4 * 1024) = ca[4]; SCHED();
    CV_MM(3, 2, 3); SCHED();
    CV_MM(3, 3, 2); SCHED();
    CV_MM(3, 4, 1); SCHED();
    CV_MM(3, 5, 0); SCHED();
    CV_MM(3, 0, 6); SCHED();
    CV_MM(3, 1, 5); SCHED(); *(LAS f32x4*)(rb + 5 * 1024) = ca[5]; SCHED();
    CV_MM(3, 2, 4); SCHED();
    CV_MM(3, 3, 3); SCHED();
    CV_MM(3, 4, 2); SCHED();
    CV_MM(3, 5, 1); SCHED();
    CV_MM(3, 6, 0); SCHED();
    CV_MM(3, 0, 7); SCHED();
    CV_MM(3, 1, 6); SCHED(); *(LAS f32x4*)(rb + 6 * 1024) = ca[6]; SCHED();
    CV_MM(3, 2, 5); SCHED();
    CV_MM(3, 3, 4); SCHED();
    CV_MM(3, 4, 3); SCHED();
    CV_MM(3, 5, 2); SCHED();
    CV_MM(3, 6, 1); SCHED();
    CV_MM(3, 7, 0); SCHED();
    *(LAS f32x4*)(rb + 7 * 1024) = ca[7];
  }
  v2u hfl;
  unsigned ph;
  {
    FRESH_IDS();
    const int c_lo = (7 * blk) >> 2, c_hi = (7 * blk + 6) >> 2;
    int cc = c_lo + (lane & 3);
    cc = cc > c_hi ? c_hi : cc;
    ph = (unsigned)(WS_FH + ((st >> 1) * 16 + cc) * FSTR);
    GLD2_SC1(hfl, ph, ws, 0);
  }
  STAMP(14);
  BAR();
  STAMP(15);
  {
    FRESH_IDS();
    const int ry = (lane & 3) * 256 + (((lane >> 2) ^ ((lane & 3) << 2)) * 16);
    const f32x4 c0 = *(LAS f32x4*)(L + L_RED + w * 1024 + ry) + *(LAS f32x4*)(L + L_RED + 8192 + w * 1024 + ry);
    const f32x4 c1 = *(LAS f32x4*)(L + L_RED + 16384 + w * 1024 + ry) + *(LAS f32x4*)(L + L_RED + 24576 + w * 1024 + ry);
    const unsigned so = (unsigned)(256 * w + 4 * lane);
    const v2u k0 = *(LAS v2u*)(L + L_U + 480 + so * 2), k1 = *(LAS v2u*)(L + L_U + 4608 + 480 + so * 2);
    const float D0 = blo(dd), D1 = bhi(dd);
    const v4u yv = v4u{pk_bf16(c0[0] + blo(k0.x) * D0, c1[0] + blo(k1.x) * D1), pk_bf16(c0[1] + bhi(k0.x) * D0, c1[1] + bhi(k1.x) * D1),
                       pk_bf16(c0[2] + blo(k0.y) * D0, c1[2] + blo(k1.y) * D1), pk_bf16(c0[3] + bhi(k0.y) * D0, c1[3] + bhi(k1.y) * D1)};
    const unsigned yo = so * 4;
    GST4_SC1(yo, yv, ws + WS_Y2 + (size_t)dpair * 8192, 0);
    WAITV(1);
    TIE(hfl);
  }
  STAMP(16);
#define COUNT_Y()                                                                   \
  do {                                                                              \
    if (COUNT_LAST(0, 7u) && (threadIdx.x & 63) == 0) {                             \
      const unsigned fo = (unsigned)(WS_FY + (n * 32 + st) * FSTR);                 \
      FLAG_ST(fo, tagv, ws);                                                        \
    }                                                                               \
  } while (0)
  const bool slow = !all_eq(hfl, tlo, thi);
  if (slow) {
    asm volatile("global_load_dwordx2 %0, %1, %2 sc1 ; SPIN\n\ts_waitcnt vmcnt(0) ; SPIN" : "=&v"(hfl) : "v"(ph), "s"(ws) : "memory");
    COUNT_Y();
    spin_flags(hfl, ph, ws, tlo, thi);
  }
  STAMP(17);
  v4u hf[HWIN];
  const char* const hb = ws + WS_H + (size_t)(4 * st * 56 + 7 * blk) * 1024 + 3072;
#define H_LOAD(x) GLD4_SC1(hf[(x) % HWIN], vl16, hb + ((((w >> 1) + 1 + (x) / 7) & 3) * 57344), ((x) % 7) * 1024 - 3072)
#pragma unroll
  for (int x = 0; x < HWIN; ++x) H_LOAD(x);
  STAMP(18);
  v4u w2f[28];
#pragma unroll
  for (int m_ = 0; m_ < 112; ++m_) TIE(w2r[m_]);
#pragma unroll
  for (int kl = 0; kl < 7; ++kl) {
#pragma unroll
    for (int hf_ = 0; hf_ < 2; ++hf_)
#pragma unroll
      for (int e2 = 0; e2 < 4; ++e2) {
        const unsigned lo_ = w2r[kl * 16 + 4 * e2 + hf_], hi_ = w2r[kl * 16 + 4 * e2 + 2 + hf_];
        w2f[kl * 4 + 2 * hf_][e2] = __builtin_amdgcn_perm(hi_, lo_, 0x05040100u);
        w2f[kl * 4 + 2 * hf_ + 1][e2] = __builtin_amdgcn_perm(hi_, lo_, 0x07060302u);
      }
#pragma unroll
    for (int j = 0; j < 4; ++j) TIE(w2f[kl * 4 + j]);
  }
  STAMP(28);
  unsigned pw, pr;
  {
    FRESH_IDS();
    pw = (unsigned)(w * 16384 + g * 256 + ((li ^ (g << 2)) * 16));
    const int r8 = lane >> 3, c8 = lane & 7, gg = c8 & 3;
    pr = (unsigned)((w >> 1) * 4096 + (c8 >> 2) * 2048 + gg * 256 + (((8 * (w & 1) + r8) ^ (gg << 2)) * 16));
  }
  v2u yf;
  unsigned py;
  v4u yt, qr;
  float gv;
  f32x4 a2[4], b0, b1;
#pragma unroll
  for (int x = 0; x < 28; ++x) {
    WAITV(g2_wait(x));
    TIE(hf[x % HWIN]);
    if (x == 0 && !slow) COUNT_Y();
    if (x == 0) STAMP(19);
    f32x4 r0, r1;
    if (x >= 21 && x < 27) {
      LAS char* const p_ = L + pr + ((((w & 6) + 2 + (x - 21)) & 7) * 16384);
      r0 = *(LAS f32x4*)(p_);
      r1 = *(LAS f32x4*)(p_ + 1024);
    }
    if (x % 7 == 0) {
#pragma unroll
      for (int j = 0; j < 4; ++j) a2[j] = f32x4{0.f, 0.f, 0.f, 0.f};
    }
    SCHED();
#pragma unroll
    for (int j = 0; j < 4; ++j) a2[j] = mfma16(w2f[(x % 7) * 4 + j], hf[x % HWIN], a2[j]);
    SCHED();
    if (x == 21) { b0 = r0; b1 = r1; }
    if (x > 21 && x < 27) { b0 += r0; b1 += r1; TIE(b0); TIE(b1); }
    if (x + HWIN < 28) H_LOAD(x + HWIN);
    if (x == XPOLL) {
      FRESH_IDS();
      py = lane < 32 ? (unsigned)(WS_FY + (n * 32 + 4 * w + (lane & 3)) * FSTR) : (unsigned)(WS_F0 + ((lane & 7) * 32 + st) * FSTR);
      GLD2_SC1(yf, py, ws, 0);
    }
    if (x == XCHK) {
      FRESH_IDS();
#pragma unroll
      for (int j = 0; j < 4; ++j) TIE(a2[j]);
      WAITV(g2_wait_poll());
      TIE(yf);
      STAMP(20);
      spin_flags(yf, py, ws, tlo, thi);
      STAMP(21);
      const unsigned yo = (unsigned)(g * 8192 + li * 16);
      GLD4_SC1(yt, yo, ws + WS_Y2 + (size_t)(32 * n + 4 * w) * 8192 + st * 256, 0);
      const unsigned go = (unsigned)((lane & 7) * 256 + (lane >> 3) * 4);
      GLD1_SC1(gv, go, ws + WS_GP + st * 2048 + w * 32, 0);
      const unsigned qo = (unsigned)((lane >> 3) * 1024 + (lane & 7) * 16);
      GLD4(qr, qo, q + (size_t)(64 * st + 8 * w) * 1024 + n * 128, 0);
    }
    if (x == 5) BAR();
    if (x % 7 == 6) {
      LAS char* const pb_ = L + pw + ((((w >> 1) + 1 + x / 7) & 3) * 4096);
#pragma unroll
      for (int j = 0; j < 4; ++j) *(LAS f32x4*)(pb_ + j * 1024) = a2[j];
    }
    if (x == 20) { BAR(); STAMP(22); }
    SCHED();
  }
  STAMP(23);
  float gt;
  {
    FRESH_IDS();
    WAITV(0);
    TIE(yt); TIE(gv); TIE(qr);
    *(LAS v4u*)(L + L_YT + (4 * w + g) * 256 + w * 16 + li * 16) = yt;
    gt = sigmoidf_(sum8(gv));
  }
  STAMP(24);
  BAR();
  STAMP(25);
  {
    FRESH_IDS();
    const int r8 = lane >> 3, c8 = lane & 7;
    LAS char* const p_ = L + pr + (w & 6) * 16384;
    b0 += *(LAS f32x4*)(p_) + *(LAS f32x4*)(p_ + 16384);
    b1 += *(LAS f32x4*)(p_ + 1024) + *(LAS f32x4*)(p_ + 16384 + 1024);
    unsigned yv[4];
#pragma unroll
    for (int i = 0; i < 4; ++i) yv[i] = *(LAS unsigned*)(L + L_YT + c8 * 1040 + i * 256 + (8 * w + r8) * 4);
    const float og = 1.0f - gt;
    v4u o;
#pragma unroll
    for (int i = 0; i < 4; ++i)
      o[i] = pk_bf16((gt * blo(qr[i])) * blo(yv[i]) + og * b0[i], (gt * bhi(qr[i])) * bhi(yv[i]) + og * b1[i]);
    const unsigned oo = (unsigned)(r8 * 1024 + c8 * 16);
    asm volatile(".Lpf_end%0:" ::"i"((g1 ? 2 : 0) + ROLE));
    GST4(oo, o, out + (size_t)(64 * st + 8 * w) * 1024 + n * 128, 0);
  }
}

extern "C" NOPK __global__ __launch_bounds__(512, 2) void fused_kernel(const char* __restrict__ q, const char* __restrict__ k, const char* __restrict__ v,
                                                       const char* __restrict__ sk, const char* __restrict__ ssmk, const char* __restrict__ ssmD,
                                                       const char* __restrict__ w1, const char* __restrict__ w2, const char* __restrict__ gw,
                                                       char* out, char* ws) {
#define TE
  unsigned cpf = 0;
  const int w = __builtin_amdgcn_readfirstlane(threadIdx.x >> 6);
  if (__builtin_expect((blockIdx.x >> 3) < 28, 1)) {
    if (w < 4) body<true, 0>(q, k, v, sk, ssmk, ssmD, w1, w2, gw, out, ws, w, cpf TE);
    else body<true, 1>(q, k, v, sk, ssmk, ssmD, w1, w2, gw, out, ws, w, cpf TE);
  } else {
    if (w < 4) body<false, 0>(q, k, v, sk, ssmk, ssmD, w1, w2, gw, out, ws, w, cpf TE);
    else body<false, 1>(q, k, v, sk, ssmk, ssmD, w1, w2, gw, out, ws, w, cpf TE);
  }
}

extern "C" void kernel_launch(void* const* d_in, const int* in_sizes, int n_in, void* d_out, int out_size, void* d_ws, size_t ws_size,
                              hipStream_t stream) {
  (void)in_sizes; (void)n_in; (void)out_size; (void)ws_size;
  (void)hipFuncSetAttribute((const void*)fused_kernel, hipFuncAttributeMaxDynamicSharedMemorySize, LDS_BYTES);
  fused_kernel<<<256, 512, LDS_BYTES, stream>>>((const char*)d_in[0], (const char*)d_in[1], (const char*)d_in[2], (const char*)d_in[3],
                                                (const char*)d_in[4], (const char*)d_in[5], (const char*)d_in[7], (const char*)d_in[8],
                                                (const char*)d_in[9], (char*)d_out, (char*)d_ws);
}
```

```cpp
#include <hip/hip_runtime.h>
#include <stdint.h>

typedef unsigned v4u __attribute__((ext_vector_type(4)));
typedef unsigned v2u __attribute__((ext_vector_type(2)));
typedef float f32x4 __attribute__((ext_vector_type(4)));
typedef float f32x2 __attribute__((ext_vector_type(2)));
typedef short bf16x8 __attribute__((ext_vector_type(8)));
typedef short s16x4 __attribute__((ext_vector_type(4)));
typedef __bf16 bf2 __attribute__((ext_vector_type(2)));
typedef unsigned long long u64;
#define LAS __attribute__((address_space(3)))
#if defined(__HIP_DEVICE_COMPILE__) && !defined(ALLOW_PK)
#define NOPK __attribute__((target("no-packed-fp32-ops")))
#else
#define NOPK
#endif

#define CODE_PF 0
#define TOUCH 2
#define KSTAMP 0
#define SLEEP 4

#define FSTR 128
constexpr unsigned WS_F0 = 786432;
constexpr unsigned WS_FH = 851968;
constexpr unsigned WS_FY = 917504;
constexpr unsigned WS_GP = 16384;
constexpr unsigned WS_TS = 262144;
constexpr unsigned WS_KVT = 1u << 20;
constexpr unsigned WS_Y2 = 3u << 20;
constexpr unsigned WS_H = 5u << 20;

constexpr unsigned L_KB = 131072;
constexpr unsigned L_VB = 140288;
constexpr unsigned L_YT = 131072;
constexpr unsigned L_CNT = 148480;
constexpr unsigned L_FILT = 0;
constexpr unsigned L_U = 33280;
constexpr unsigned L_RED = 65536;
constexpr unsigned LDS_BYTES = 148544;

constexpr int SPIN_CAP = 1 << 16;

NOPK __device__ __forceinline__ unsigned pk_bf16(float a, float b) {
  f32x2 v = {a, b};
  return __builtin_bit_cast(unsigned, __builtin_convertvector(v, bf2));
}
NOPK __device__ __forceinline__ float blo(unsigned u) { return __builtin_bit_cast(float, u << 16); }
NOPK __device__ __forceinline__ float bhi(unsigned u) { return __builtin_bit_cast(float, u & 0xffff0000u); }
NOPK __device__ __forceinline__ f32x4 mfma16(v4u a, v4u b, f32x4 c) {
  return __builtin_amdgcn_mfma_f32_16x16x32_bf16(__builtin_bit_cast(bf16x8, a), __builtin_bit_cast(bf16x8, b), c, 0, 0, 0);
}
NOPK __device__ __forceinline__ float sigmoidf_(float x) {
  return __builtin_amdgcn_rcpf(1.0f + __builtin_amdgcn_exp2f(-1.4426950408889634f * x));
}
NOPK __device__ __forceinline__ float silu1(float x) { return x * sigmoidf_(x); }
NOPK __device__ __forceinline__ float sum8(float x) {
  x += __builtin_bit_cast(float, __builtin_amdgcn_update_dpp(0, __builtin_bit_cast(int, x), 0xB1, 0xf, 0xf, true));
  x += __builtin_bit_cast(float, __builtin_amdgcn_update_dpp(0, __builtin_bit_cast(int, x), 0x4E, 0xf, 0xf, true));
  x += __builtin_bit_cast(float, __builtin_amdgcn_update_dpp(0, __builtin_bit_cast(int, x), 0x141, 0xf, 0xf, true));
  return x;
}

#define GLD1(dst, voff, sbase, imm) asm volatile("global_load_dword %0, %1, %2 offset:%3" : "=v"(dst) : "v"(voff), "s"(sbase), "i"(imm))
#define GLD1_SC1(dst, voff, sbase, imm) asm volatile("global_load_dword %0, %1, %2 offset:%3 sc1" : "=v"(dst) : "v"(voff), "s"(sbase), "i"(imm))
#define GLD2(dst, voff, sbase, imm) asm volatile("global_load_dwordx2 %0, %1, %2 offset:%3" : "=v"(dst) : "v"(voff), "s"(sbase), "i"(imm))
#define GLD2_SC1(dst, voff, sbase, imm) asm volatile("global_load_dwordx2 %0, %1, %2 offset:%3 sc1" : "=v"(dst) : "v"(voff), "s"(sbase), "i"(imm))
#define GLD4(dst, voff, sbase, imm) asm volatile("global_load_dwordx4 %0, %1, %2 offset:%3" : "=v"(dst) : "v"(voff), "s"(sbase), "i"(imm))
#define GLD4_SC1(dst, voff, sbase, imm) asm volatile("global_load_dwordx4 %0, %1, %2 offset:%3 sc1" : "=v"(dst) : "v"(voff), "s"(sbase), "i"(imm))
#define GST4_SC1(voff, data, sbase, imm) asm volatile("global_store_dwordx4 %0, %1, %2 offset:%3 sc1\n\ts_nop 1" ::"v"(voff), "v"(data), "s"(sbase), "i"(imm) : "memory")
#define GST4(voff, data, sbase, imm) asm volatile("global_store_dwordx4 %0, %1, %2 offset:%3 nt\n\ts_nop 1" ::"v"(voff), "v"(data), "s"(sbase), "i"(imm) : "memory")
#define FLAG_ST(voff, data, sbase) asm volatile("global_store_dwordx2 %0, %1, %2 sc1 ; FLAG" ::"v"(voff), "v"(data), "s"(sbase) : "memory")
#define GST1_SC1(voff, data, sbase, imm) asm volatile("global_store_dword %0, %1, %2 offset:%3 sc1" ::"v"(voff), "v"(data), "s"(sbase), "i"(imm) : "memory")
#define DMA16(ldsaddr, voff, sbase) asm volatile("s_mov_b32 m0, %0\n\ts_nop 0\n\tglobal_load_lds_dwordx4 %1, %2" ::"s"(ldsaddr), "v"(voff), "s"(sbase) : "memory")
#define WAITV(n) asm volatile("s_waitcnt vmcnt(%0)" ::"i"(n) : "memory")
#define BAR() asm volatile("s_waitcnt lgkmcnt(0)\n\ts_barrier" ::: "memory")
#define TIE(x) asm volatile("" : "+v"(x))
#define SCHED() __builtin_amdgcn_sched_barrier(0)
#define FRESH_IDS()                                         \
  int tid = threadIdx.x;                                    \
  asm volatile("" : "+v"(tid));                             \
  const int lane = tid & 63, li = lane & 15, g = lane >> 4; \
  (void)li; (void)g;

#define STAMP(i) do {} while (0)

#define NOSYNC 0
NOPK __device__ __forceinline__ bool all_eq(v2u f, unsigned tlo, unsigned thi) {
  if (NOSYNC) return true;
  return __builtin_amdgcn_ballot_w64(f.x == tlo && f.y == thi) == ~0ull;
}
NOPK __device__ __forceinline__ void spin_flags(v2u f, unsigned voff, const char* ws, unsigned tlo, unsigned thi) {
  int spins = 0;
  while (__builtin_expect(!all_eq(f, tlo, thi), 0)) {
    if (++spins > SPIN_CAP) break;
    __builtin_amdgcn_s_sleep(SLEEP);
    asm volatile("global_load_dwordx2 %0, %1, %2 sc1 ; SPIN\n\ts_waitcnt vmcnt(0) ; SPIN" : "=&v"(f) : "v"(voff), "s"(ws) : "memory");
  }
}

#define HWIN 8
#define XPOLL 14
#define XCHK 20
#define DPRE_A 12
#define F0_IT 2
#define W2S 4
#define W2D 32
NOPK __host__ __device__ __forceinline__ constexpr int g2_issued(int x) {
  int c = HWIN;
  for (int t = 0; t < x; ++t) c += (t + HWIN < 28 ? 1 : 0) + (t == XPOLL ? 1 : 0) + (t == XCHK ? 3 : 0);
  return c;
}
NOPK __host__ __device__ __forceinline__ constexpr int g2_pos_h(int j) { return j < HWIN ? j : g2_issued(j - HWIN); }
NOPK __host__ __device__ __forceinline__ constexpr int g2_wait(int x) { return g2_issued(x) - g2_pos_h(x) - 1; }
NOPK __host__ __device__ __forceinline__ constexpr int g2_wait_poll() { return (g2_issued(XCHK) + (XCHK + HWIN < 28 ? 1 : 0)) - (g2_issued(XPOLL) + (XPOLL + HWIN < 28 ? 1 : 0)) - 1; }
template <bool g1, int ROLE>
NOPK __device__ __forceinline__ void body(const char* __restrict__ q, const char* __restrict__ k, const char* __restrict__ v,
                                     const char* __restrict__ sk, const char* __restrict__ ssmk, const char* __restrict__ ssmD,
                                     const char* __restrict__ w1, const char* __restrict__ w2, const char* __restrict__ gw,
                                     char* out, char* ws, const int w, unsigned cpf
) {
  extern __shared__ __attribute__((aligned(16))) char smem_[];
  LAS char* const L = (LAS char*)smem_;
  const unsigned lbase = (unsigned)(uintptr_t)L;
  constexpr bool CW = ROLE == 0, HW = ROLE == 1;
  const int id = blockIdx.x, xcd = id & 7, loc = id >> 3;
  const int st = 4 * xcd + (loc >> 3), n = loc & 7;
  const int R = 4 * (xcd >> 1) + (g1 ? loc / 7 : 0), c = 7 * (xcd & 1) + loc % 7;
  const int dpair = 32 * n + st;
  const int j4 = w & 3;
#define BLKROT (n + 2 * (loc >> 3))
  const int blk = (w + BLKROT) & 7;

  if (HW) __builtin_amdgcn_s_setprio(3);
  u64 pfpc = 0; unsigned pfo = 0; (void)pfpc; (void)pfo;
  constexpr bool G1C = g1 && CW;
  int st8 = st * 8;
  asm volatile("" : "+s"(st8));
  unsigned vx0 = 0, vx1 = 0, vw0 = 0, vw1 = 0;
  const char* const xb = q + (size_t)R * 131072 + j4 * 32768;
  const char* const wb = w1 + c * 256 + j4 * 57344;
  if (G1C) {
    FRESH_IDS();
    vx0 = (unsigned)((lane >> 3) * 1024 + (((lane & 7) ^ g) * 16));
    vx1 = (unsigned)((8 + (lane >> 3)) * 1024 + (((lane & 7) ^ (4 + g)) * 16));
    vw0 = (unsigned)(g * 3584 + (((li >> 1) ^ g) * 32) + (li & 1) * 16);
    vw1 = (unsigned)((8 + g) * 3584 + (((li >> 1) ^ (g | 4)) * 32) + (li & 1) * 16);
  }
#define ROTK 0
  const int rot = ROTK;
#define DMA_PIECE(ch, p)                                                                                      \
  do {                                                                                                        \
    if (G1C) {                                                                                                \
      const unsigned sl_ = lbase + ((ch) & 3) * 32768 + j4 * 4096;                                            \
      const int kc_ = ((ch) + rot) & 7;                                                                       \
      if ((p) == 0) DMA16(sl_, vx0, xb + kc_ * 128);                                                          \
      if ((p) == 1) DMA16(sl_ + 16384, vw0, wb + kc_ * 229376);                                               \
      if ((p) == 2) DMA16(sl_ + 1024, vx1, xb + kc_ * 128);                                                   \
      if ((p) == 3) DMA16(sl_ + 16384 + 1024, vw0, wb + kc_ * 229376 + 14336);                                \
      if ((p) == 4) DMA16(sl_ + 2048, vx0, xb + kc_ * 128 + 16384);                                           \
      if ((p) == 5) DMA16(sl_ + 16384 + 2048, vw1, wb + kc_ * 229376);                                        \
      if ((p) == 6) DMA16(sl_ + 3072, vx1, xb + kc_ * 128 + 16384);                                           \
      if ((p) == 7) DMA16(sl_ + 16384 + 3072, vw1, wb + kc_ * 229376 + 14336);                                \
    }                                                                                                         \
  } while (0)
#define DMA_RANGE(lo, hi) _Pragma("unroll") for (int d_ = (lo); d_ < (hi); ++d_) DMA_PIECE(d_ >> 3, d_ & 7)
  DMA_RANGE(0, 8);
  unsigned cpf2 = 0;
  DMA_RANGE(8, DPRE_A);

  u64 epoch;
  unsigned dd;
  asm volatile("s_load_dwordx2 %0, %1, 0x0" : "=s"(epoch) : "s"(ws));
  asm volatile("s_load_dword %0, %1, 0x0" : "=s"(dd) : "s"(ssmD + dpair * 4));

  unsigned vl16;
  { FRESH_IDS(); vl16 = (unsigned)(lane * 16); }
  v4u fhi[2], skr, qa, qb, gwr;
  v2u flo[2];
  if (HW) {
    FRESH_IDS();
    const unsigned ro = (unsigned)((lane >> 3) * 1024);
    const char* const kb_ = k + (size_t)st * 65536 + n * 128 + j4 * 16384;
    const char* const vb_ = v + (size_t)st * 65536 + n * 128 + j4 * 16384;
    const unsigned ka = ro + (((lane & 7) ^ ((2 * j4 + 1) & 7)) * 16), kc = ro + (((lane & 7) ^ ((2 * j4 + 2) & 7)) * 16);
    const unsigned va = ro + (((lane & 7) ^ (2 * j4)) * 16);
    const unsigned qo = ro + (lane & 7) * 16;
    DMA16(lbase + L_KB + 1024 + j4 * 2048, ka, kb_);
    DMA16(lbase + L_KB + 2048 + j4 * 2048, kc, kb_ + 8192);
    DMA16(lbase + L_VB + j4 * 2048, va, vb_);
    DMA16(lbase + L_VB + 1024 + j4 * 2048, ka, vb_ + 8192);
    if (j4 == 0) DMA16(lbase + L_KB, qo, kb_ - (st > 0 ? 8192 : 0));
    const unsigned so = (unsigned)((lane >> 3) * 16);
    GLD4(skr, so, sk + n * 512 + j4 * 128, 0);
    const char* const qb_ = q + (size_t)st * 65536 + n * 128 + j4 * 16384;
    GLD4(qa, qo, qb_, 0);
    GLD4(qb, qo, qb_ + 8192, 0);
    const unsigned go = (unsigned)((lane & 7) * 16);
    GLD4(gwr, go, gw + n * 128, 0);
    if (tid == 256) *(LAS v4u*)(L + L_CNT) = v4u{0u, 0u, 0u, 0u};
    {
      const char* tb_ = j4 == 0 ? w1 + 688128 + (size_t)id * 4480 : j4 == 1 ? q + (size_t)id * 8192 + 384 : j4 == 2 ? w2 + (size_t)id * 7168 : ssmk + (size_t)dpair * 8192;
      const int lim = j4 == 0 ? 34 : j4 == 1 ? 39 : j4 == 2 ? 55 : 63;
      const int tl = lane < lim ? lane : lim;
      const unsigned to = j4 == 1 ? (unsigned)((tl / 5) * 1024 + (tl % 5) * 128) : (unsigned)(tl * 128);
      GLD1(cpf, to, tb_, 0);
      if (TOUCH == 2) GLD1(cpf2, to, tb_, 64);
    }
    WAITV(3 + TOUCH);
  }
  if (CW) STAMP(1);
  BAR();
  STAMP(2);
  asm volatile("" : "+s"(epoch), "+s"(dd));
  const u64 tag = epoch * 6364136223846793005ull + 1442695040888963407ull;
  const unsigned tlo = (unsigned)tag, thi = (unsigned)(tag >> 32);
  const v2u tagv = v2u{tlo, thi};
  DMA_RANGE(DPRE_A, 24);
  if (G1C) WAITV(16 + (CODE_PF ? 2 : 0));
#define KV_HALF(SEL, ta, tb, off)                                                                                          \
  do {                                                                                                                    \
    float kf[11], r_[8];                                                                                                  \
    const float t0 = blo(ta), t1 = bhi(ta), t2 = blo(tb), t3 = bhi(tb);                                                   \
    _Pragma("unroll") for (int i = 0; i < 11; ++i) kf[i] = SEL(kk[i]);                                                    \
    _Pragma("unroll") for (int i = 0; i < 8; ++i) r_[i] = (kf[i + 3] * t0 + kf[i + 2] * t1 + kf[i + 1] * t2 + kf[i] * t3) * SEL(vv[i]); \
    const v4u o_ = v4u{pk_bf16(r_[0], r_[1]), pk_bf16(r_[2], r_[3]), pk_bf16(r_[4], r_[5]), pk_bf16(r_[6], r_[7])};        \
    GST4_SC1(ko, o_, ws + WS_KVT + (size_t)n * 262144 + st * 128 + (off), 0);                                             \
  } while (0)
  if (HW) {
    FRESH_IDS();
    TIE(skr);
    const int sg = lane & 7, dp = 8 * j4 + (lane >> 3);
    const unsigned cb = (unsigned)((dp & 3) * 4), ck = (unsigned)(dp >> 2);
    unsigned kk[11], vv[8];
#pragma unroll
    for (int i = 0; i < 3; ++i) kk[i] = *(LAS unsigned*)(L + L_KB + (8 * sg + 5 + i) * 128 + ((ck ^ sg) * 16) + cb);
#pragma unroll
    for (int i = 0; i < 8; ++i) kk[3 + i] = *(LAS unsigned*)(L + L_KB + (8 * sg + 8 + i) * 128 + ((ck ^ ((sg + 1) & 7)) * 16) + cb);
#pragma unroll
    for (int i = 0; i < 8; ++i) vv[i] = *(LAS unsigned*)(L + L_VB + (8 * sg + i) * 128 + ((ck ^ sg) * 16) + cb);
    if (st == 0 && sg == 0) { kk[0] = 0u; kk[1] = 0u; kk[2] = 0u; }
    const unsigned ko = (unsigned)(dp * 8192 + sg * 16);
    KV_HALF(blo, skr.x, skr.y, 0);
    KV_HALF(bhi, skr.z, skr.w, 4096);
    SCHED();
  }
  if (!g1 && CW) WAITV(0);
  BAR();
  STAMP(3);
  if (HW) {
    FRESH_IDS();
    WAITV(2);
    TIE(qa); TIE(qb); TIE(gwr);
    float ga = 0.f, gb = 0.f;
#pragma unroll
    for (int i = 0; i < 4; ++i) {
      ga += blo(qa[i]) * blo(gwr[i]) + bhi(qa[i]) * bhi(gwr[i]);
      gb += blo(qb[i]) * blo(gwr[i]) + bhi(qb[i]) * bhi(gwr[i]);
    }
    ga = sum8(ga);
    gb = sum8(gb);
    const float gs = (lane & 1) ? gb : ga;
    const unsigned gpo = (unsigned)(((lane >> 3) + (lane & 1) * 8) * 4);
    GST1_SC1(gpo, gs, ws + WS_GP + (st8 + n) * 256 + j4 * 64, 0);
    __builtin_amdgcn_s_setprio(0);
    SCHED();
  }

  f32x4 acc1[4][4];
#pragma unroll
  for (int a = 0; a < 4; ++a)
#pragma unroll
    for (int b = 0; b < 4; ++b) acc1[a][b] = f32x4{0.f, 0.f, 0.f, 0.f};
  const int wsx = w & 1, wfx = (w >> 1) & 1;
  unsigned ax0 = 0, aw0 = 0;
  if (G1C) {
    FRESH_IDS();
    __builtin_amdgcn_s_setprio(1);
    ax0 = (unsigned)((wsx * 64 + li) * 128 + ((g ^ (li >> 1)) * 16));
    const unsigned sg = (unsigned)((li >> 2) | ((g & 1) << 2));
    aw0 = (unsigned)(16384 + (8 * g + (li >> 2)) * 256 + (((4 * wfx) ^ sg) * 32) + (lane & 3) * 8);
  }
  v4u xf[2][4];
  v2u wfa[2][4], wfb[2][4];
#define RD_WA(hs, gq) wfa[(hs) & 1][gq] = __builtin_bit_cast(v2u, __builtin_amdgcn_ds_read_tr16_b64_v4i16((LAS s16x4*)(L + (((hs) >> 1) & 3) * 32768 + (aw0 ^ ((gq) * 32)) + ((hs) & 1) * 8192)))
#define RD_WB(hs, gq) wfb[(hs) & 1][gq] = __builtin_bit_cast(v2u, __builtin_amdgcn_ds_read_tr16_b64_v4i16((LAS s16x4*)(L + (((hs) >> 1) & 3) * 32768 + (aw0 ^ ((gq) * 32)) + ((hs) & 1) * 8192 + 1024)))
#define RD_X(hs, gq) xf[(hs) & 1][gq] = *(LAS v4u*)(L + (((hs) >> 1) & 3) * 32768 + (ax0 ^ (((hs) & 1) * 64)) + (gq) * 2048)
#define MM1(hs, t, gq) acc1[t][gq] = mfma16(v4u{wfa[(hs) & 1][t].x, wfa[(hs) & 1][t].y, wfb[(hs) & 1][t].x, wfb[(hs) & 1][t].y}, xf[(hs) & 1][gq], acc1[t][gq])
#define MM_GROUP(hs, gq) _Pragma("unroll") for (int t_ = 0; t_ < 4; ++t_) MM1(hs, t_, gq)

  unsigned w2r[112], vw2;
  { FRESH_IDS(); vw2 = (unsigned)(g * 4096 + li * 4); }
  const char* const w2b = w2 + (size_t)blk * 229376 + n * 128;
#define W2_LOAD(m) GLD1(w2r[m], vw2, w2b + ((m) >> 3) * 16384, (((m) >> 1) & 3) * 1024 + ((m) & 1) * 64)
#define W2_RANGE(lo, hi) _Pragma("unroll") for (int m_ = (lo); m_ < (hi); ++m_) { if (m_ >= 40 && (m_ & 3) == 0) WAITV(56); W2_LOAD(m_); }

#define HALF_STEP(hs, dlo, dhi)                                                                      \
  do {                                                                                               \
    if (G1C) {                                                                                       \
      _Pragma("unroll") for (int u_ = 0; u_ < 16; ++u_) {                                            \
        MM1(hs, u_ & 3, u_ >> 2);                                                                    \
        SCHED();                                                                                     \
        if (u_ == 0) RD_X((hs) + 1, 0);                                                              \
        else if (u_ < 9) { if (u_ & 1) RD_WA((hs) + 1, (u_ - 1) >> 1); else RD_WB((hs) + 1, (u_ - 1) >> 1); } \
        else if (u_ < 12) RD_X((hs) + 1, u_ - 8);                                                    \
        if ((u_ & 3) == 3) { DMA_RANGE((dlo) + (((dhi) - (dlo)) * (u_ >> 2)) / 4, (dlo) + (((dhi) - (dlo)) * ((u_ >> 2) + 1)) / 4); } \
        SCHED();                                                                                     \
      }                                                                                              \
    }                                                                                                \
  } while (0)
#define PIN_ACC1() do { if (G1C) { _Pragma("unroll") for (int a_ = 0; a_ < 16; ++a_) TIE(acc1[a_ & 3][a_ >> 2]); } } while (0)
  v2u pf;
  unsigned pf0;
  v4u kvt[2];
#define G1_HEAD(it, ncw)                                                                             \
  do {                                                                                               \
    PIN_ACC1();                                                                                      \
    if (G1C) WAITV(ncw);                                                                             \
    BAR();                                                                                           \
    STAMP(4 + (it));                                                                                 \
  } while (0)
#define F0_FLAG()                                                          \
  do {                                                                     \
    if (HW && threadIdx.x == 256) {                                        \
      const unsigned fo = (unsigned)(WS_F0 + (n * 32 + st) * FSTR);        \
      FLAG_ST(fo, tagv, ws);                                               \
    }                                                                      \
  } while (0)
  if (G1C) {
#pragma unroll
    for (int gq = 0; gq < 4; ++gq) { RD_WA(0, gq); RD_WB(0, gq); RD_X(0, gq); }
    SCHED();
  }
  HALF_STEP(0, 24, 32);
  G1_HEAD(0, 16);
  TIE(cpf); TIE(cpf2);
  HALF_STEP(1, 32, 36); HALF_STEP(2, 36, 40);
  if (HW && F0_IT == 1) WAITV(0);
  G1_HEAD(1, 16);
  if (F0_IT == 1) F0_FLAG();
  HALF_STEP(3, 40, 44); HALF_STEP(4, 44, 48);
  if (HW && F0_IT == 2) WAITV(0);
  G1_HEAD(2, 16);
  if (F0_IT == 2) F0_FLAG();
  HALF_STEP(5, 48, 52); HALF_STEP(6, 52, 56);
  G1_HEAD(3, 16);
  if (HW) {
    FRESH_IDS();
    const int bi = tid & 255;
    const unsigned fo = (unsigned)((255 - bi) * 16);
    const unsigned fo2 = bi == 255 ? fo : fo - 8;
    const char* const fb_ = ssmk + (size_t)(2 * dpair) * 4096;
    GLD4(fhi[0], fo, fb_, 0);
    GLD2(flo[0], fo2, fb_, 0);
    GLD4(fhi[1], fo, fb_ + 4096, 0);
    GLD2(flo[1], fo2, fb_ + 4096, 0);
  }
  HALF_STEP(7, 56, 60); HALF_STEP(8, 60, 64);
  G1_HEAD(4, 16);
  HALF_STEP(9, 0, 0); HALF_STEP(10, 0, 0);
  if (HW) { W2_RANGE(0, 28); }
  G1_HEAD(5, 8);
  HALF_STEP(11, 0, 0); HALF_STEP(12, 0, 0);
  if (HW) {
    FRESH_IDS();
    pf0 = (unsigned)(WS_F0 + (n * 32 + 8 * j4 + (lane & 7)) * FSTR);
    GLD2_SC1(pf, pf0, ws, 0);
    W2_RANGE(28, 56);
  }
  G1_HEAD(6, 0);
  HALF_STEP(13, 0, 0); HALF_STEP(14, 0, 0);
  STAMP(11);

#define COUNT_LAST(idx, last) \
  (__builtin_amdgcn_readfirstlane((threadIdx.x & 63) == 0 ? __hip_atomic_fetch_add((LAS unsigned*)(L + L_CNT + 4 * (idx)), 1u, __ATOMIC_RELAXED, __HIP_MEMORY_SCOPE_WORKGROUP) : 0u) == (last))
  if (CW) {
    char* const hb_ = ws + WS_H + (size_t)((8 * R + 4 * wsx) * 56 + 4 * c + 2 * wfx) * 1024;
#define SILU_TILE(i, kp)                                                                                               \
  do {                                                                                                                 \
    if (g1) {                                                                                                          \
    float s_[8];                                                                                                       \
    _Pragma("unroll") for (int t2 = 0; t2 < 2; ++t2)                                                                   \
      _Pragma("unroll") for (int r = 0; r < 4; ++r) s_[t2 * 4 + r] = silu1(acc1[2 * (kp) + t2][i][r]);                 \
    const v4u hv = v4u{pk_bf16(s_[0], s_[1]), pk_bf16(s_[2], s_[3]), pk_bf16(s_[4], s_[5]), pk_bf16(s_[6], s_[7])};    \
    GST4_SC1(vl16, hv, hb_ + (i) * 57344, (kp) * 1024);                                                                \
    }                                                                                                                  \
    SCHED();                                                                                                           \
  } while (0)
    if (g1) { MM_GROUP(15, 0); SCHED(); MM_GROUP(15, 1); SCHED(); }
    SILU_TILE(0, 0); W2_RANGE(0 * W2S, 1 * W2S); SILU_TILE(0, 1); W2_RANGE(1 * W2S, 2 * W2S);
    if (g1) { SCHED(); MM_GROUP(15, 2); SCHED(); }
    SILU_TILE(1, 0); W2_RANGE(2 * W2S, 3 * W2S); SILU_TILE(1, 1); W2_RANGE(3 * W2S, 4 * W2S);
    if (g1) { SCHED(); MM_GROUP(15, 3); SCHED(); }
    SILU_TILE(2, 0); W2_RANGE(4 * W2S, 5 * W2S); SILU_TILE(2, 1); W2_RANGE(5 * W2S, 6 * W2S);
    SCHED();
    SILU_TILE(3, 0); W2_RANGE(6 * W2S, 7 * W2S); SILU_TILE(3, 1);
    __builtin_amdgcn_s_setprio(0);
    STAMP(12);
  } else {
    FRESH_IDS();
    WAITV(28);
    TIE(pf);
    spin_flags(pf, pf0, ws, tlo, thi);
    STAMP(26);
    GLD4_SC1(kvt[0], vl16, ws + WS_KVT + (size_t)(2 * dpair) * 4096 + j4 * 1024, 0);
    GLD4_SC1(kvt[1], vl16, ws + WS_KVT + (size_t)(2 * dpair + 1) * 4096 + j4 * 1024, 0);
    W2_RANGE(56, 84);
    const int bi = tid & 255;
#pragma unroll
    for (int ch = 0; ch < 2; ++ch) {
      TIE(fhi[ch]); TIE(flo[ch]);
      if (bi == 255) flo[ch] = v2u{0u, 0u};
      const unsigned W2_ = flo[ch].x, W3 = flo[ch].y, W4 = fhi[ch].x, W5 = fhi[ch].y, W6 = fhi[ch].z, W7 = fhi[ch].w;
      const unsigned S3 = __builtin_amdgcn_alignbit(W3, W3, 16), S4 = __builtin_amdgcn_alignbit(W4, W4, 16), S5 = __builtin_amdgcn_alignbit(W5, W5, 16),
                     S6 = __builtin_amdgcn_alignbit(W6, W6, 16), S7 = __builtin_amdgcn_alignbit(W7, W7, 16);
      const unsigned T3 = (W3 & 0xffffu) | (W2_ & 0xffff0000u), T4 = (W4 & 0xffffu) | (W3 & 0xffff0000u), T5 = (W5 & 0xffffu) | (W4 & 0xffff0000u),
                     T6 = (W6 & 0xffffu) | (W5 & 0xffff0000u), T7 = (W7 & 0xffffu) | (W6 & 0xffff0000u);
      LAS char* fb = L + L_FILT + ch * 16640 + bi * 16;
      *(LAS v4u*)(fb) = v4u{S7, S6, S5, S4};
      *(LAS v4u*)(fb + 4160) = v4u{T7, T6, T5, T4};
      *(LAS v4u*)(fb + 8320) = v4u{S6, S5, S4, S3};
      *(LAS v4u*)(fb + 12480) = v4u{T6, T5, T4, T3};
    }
    if (j4 < 2) {
      const unsigned za = lane < 32 ? (unsigned)(L_U + j4 * 4608 + (lane < 30 ? lane : 29) * 16)
                                    : (unsigned)(L_FILT + j4 * 16640 + ((lane >> 1) & 3) * 4160 + 4096 + (lane & 1) * 16);
      *(LAS v4u*)(L + za) = v4u{0u, 0u, 0u, 0u};
    }
    W2_RANGE(84, 112);
    WAITV(56);
    TIE(kvt[0]); TIE(kvt[1]);
    *(LAS v4u*)(L + L_U + 480 + j4 * 1024 + vl16) = kvt[0];
    *(LAS v4u*)(L + L_U + 4608 + 480 + j4 * 1024 + vl16) = kvt[1];
    STAMP(12);
  }
  BAR();
  STAMP(13);
  if (CW) {
    W2_RANGE(7 * W2S, 7 * W2S + W2D);
    if (g1) {
      WAITV(W2D);
      if (COUNT_LAST(1, 3u) && (threadIdx.x & 63) == 0) {
        const unsigned fo = (unsigned)(WS_FH + (R * 16 + c) * FSTR);
        FLAG_ST(fo, tagv, ws);
      }
    }
    STAMP(27);
    W2_RANGE(7 * W2S + W2D, 112);
    if (!g1 && id == 8 * 28 && w == 0) {
      FRESH_IDS();
#pragma unroll 1
      for (int part = 0; part < 4; ++part) {
        const unsigned eo = (unsigned)(WS_F0 + (part * 64 + lane) * FSTR);
        spin_flags(v2u{~tlo, 0u}, eo, ws, tlo, thi);
      }
      if (lane == 0) { const unsigned z = 0; FLAG_ST(z, tagv, ws); }
    }
  } else {
    FRESH_IDS();
    const int ch = j4 >> 1, mh = j4 & 1;
    f32x4 ca[8];
#pragma unroll
    for (int i = 0; i < 8; ++i) ca[i] = f32x4{0.f, 0.f, 0.f, 0.f};
    unsigned abo = (unsigned)(L_FILT + ch * 16640 + (3 - (li & 3)) * 4160 + 2 * (2028 + 8 * g - 4 * (li >> 2)) - 512 * 7 - 256 * mh - 192);
    unsigned abo2 = abo + 8;
    TIE(abo2);
    LAS char* const bb = L + L_U + ch * 4608 + 2 * (224 + 16 * li + 8 * g) - 256 * mh - 192;
#define CV_A0(mm, i) Aa[mm][i] = *(LAS v2u*)(L + abo + (3 - (mm)) * 64 + 512 * (7 - (i)))
#define CV_A1(mm, i) Ab[mm][i] = *(LAS v2u*)(L + abo2 + (3 - (mm)) * 64 + 512 * (7 - (i)))
#define CV_B(mm, j) B[mm][j] = *(LAS v4u*)(bb + (3 - (mm)) * 64 + 512 * (j))
#define CV_MM(mm, i, j) ca[(i) + (j)] = mfma16(v4u{Aa[mm][i].x, Aa[mm][i].y, Ab[mm][i].x, Ab[mm][i].y}, B[mm][j], ca[(i) + (j)])
    v2u Aa[4][8], Ab[4][8];
    v4u B[4][8];
    CV_A0(0, 0); SCHED(); CV_A1(0, 0); SCHED();
#pragma unroll
    for (int j = 0; j < 8; ++j) CV_B(0, j);
    SCHED();
    CV_A0(0, 1); SCHED(); CV_A1(0, 1); SCHED(); CV_A0(0, 2); SCHED(); CV_A1(0, 2); SCHED();
#define CV_STEP(mm) \
    CV_MM(mm, 0, 0); SCHED(); CV_A0(mm, 3); SCHED(); \
    CV_MM(mm, 0, 1); SCHED(); CV_A1(mm, 3); SCHED(); \
    CV_MM(mm, 0, 2); SCHED(); CV_B((mm) + 1, 0); SCHED(); \
    CV_MM(mm, 0, 3); SCHED(); \
    CV_MM(mm, 0, 4); SCHED(); CV_A0(mm, 4); SCHED(); \
    CV_MM(mm, 0, 5); SCHED(); CV_A1(mm, 4); SCHED(); \
    CV_MM(mm, 0, 6); SCHED(); CV_B((mm) + 1, 1); SCHED(); \
    CV_MM(mm, 0, 7); SCHED(); \
    CV_MM(mm, 1, 0); SCHED(); CV_A0(mm, 5); SCHED(); \
    CV_MM(mm, 1, 1); SCHED(); CV_A1(mm, 5); SCHED(); \
    CV_MM(mm, 1, 2); SCHED(); CV_B((mm) + 1, 2); SCHED(); \
    CV_MM(mm, 1, 3); SCHED(); \
    CV_MM(mm, 1, 4); SCHED(); CV_A0(mm, 6); SCHED(); \
    CV_MM(mm, 1, 5); SCHED(); CV_A1(mm, 6); SCHED(); \
    CV_MM(mm, 1, 6); SCHED(); CV_A0(mm, 7); SCHED(); \
    CV_MM(mm, 2, 0); SCHED(); \
    CV_MM(mm, 2, 1); SCHED(); CV_A1(mm, 7); SCHED(); \
    CV_MM(mm, 2, 2); SCHED(); CV_B((mm) + 1, 3); SCHED(); \
    CV_MM(mm, 2, 3); SCHED(); CV_B((mm) + 1, 4); SCHED(); \
    CV_MM(mm, 2, 4); SCHED(); \
    CV_MM(mm, 2, 5); SCHED(); CV_A0((mm) + 1, 0); SCHED(); \
    CV_MM(mm, 3, 0); SCHED(); CV_A1((mm) + 1, 0); SCHED(); \
    CV_MM(mm, 3, 1); SCHED(); CV_B((mm) + 1, 5); SCHED(); \
    CV_MM(mm, 3, 2); SCHED(); \
    CV_MM(mm, 3, 3); SCHED(); CV_B((mm) + 1, 6); SCHED(); \
    CV_MM(mm, 3, 4); SCHED(); CV_B((mm) + 1, 7); SCHED(); \
    CV_MM(mm, 4, 0); SCHED(); CV_A0((mm) + 1, 1); SCHED(); \
    CV_MM(mm, 4, 1); SCHED(); \
    CV_MM(mm, 4, 2); SCHED(); CV_A1((mm) + 1, 1); SCHED(); \
    CV_MM(mm, 4, 3); SCHED(); CV_A0((mm) + 1, 2); SCHED(); \
    CV_MM(mm, 5, 0); SCHED(); CV_A1((mm) + 1, 2); SCHED(); \
    CV_MM(mm, 5, 1); SCHED(); \
    CV_MM(mm, 5, 2); SCHED(); \
    CV_MM(mm, 6, 0); SCHED(); \
    CV_MM(mm, 6, 1); SCHED(); \
    CV_MM(mm, 7, 0); SCHED(); \
    do {} while (0)
    CV_STEP(0); CV_STEP(1); CV_STEP(2);
    LAS char* const rb = L + L_RED + j4 * 8192 + g * 256 + ((li ^ (g << 2)) * 16);
    CV_MM(3, 0, 0); SCHED(); CV_A0(3, 3); SCHED();
    CV_MM(3, 0, 1); SCHED(); CV_A1(3, 3); SCHED();
    CV_MM(3, 1, 0); SCHED(); CV_A0(3, 4); SCHED();
    CV_MM(3, 0, 2); SCHED(); CV_A1(3, 4); SCHED(); *(LAS f32x4*)(rb) = ca[0]; SCHED();
    CV_MM(3, 1, 1); SCHED(); CV_A0(3, 5); SCHED(); *(LAS f32x4*)(rb + 1 * 1024) = ca[1]; SCHED();
    CV_MM(3, 2, 0); SCHED(); CV_A1(3, 5); SCHED();
    CV_MM(3, 0, 3); SCHED(); CV_A0(3, 6); SCHED();
    CV_MM(3, 1, 2); SCHED(); CV_A1(3, 6); SCHED(); *(LAS f32x4*)(rb + 2 * 1024) = ca[2]; SCHED();
    CV_MM(3, 2, 1); SCHED(); CV_A0(3, 7); SCHED();
    CV_MM(3, 3, 0); SCHED(); CV_A1(3, 7); SCHED();
    CV_MM(3, 0, 4); SCHED();
    CV_MM(3, 1, 3); SCHED(); *(LAS f32x4*)(rb + 3 * 1024) = ca[3]; SCHED();
    CV_MM(3, 2, 2); SCHED();
    CV_MM(3, 3, 1); SCHED();
    CV_MM(3, 4, 0); SCHED();
    CV_MM(3, 0, 5); SCHED();
    CV_MM(3, 1, 4); SCHED(); *(LAS f32x4*)(rb + 4 * 1024) = ca[4]; SCHED();
    CV_MM(3, 2, 3); SCHED();
    CV_MM(3, 3, 2); SCHED();
    CV_MM(3, 4, 1); SCHED();
    CV_MM(3, 5, 0); SCHED();
    CV_MM(3, 0, 6); SCHED();
    CV_MM(3, 1, 5); SCHED(); *(LAS f32x4*)(rb + 5 * 1024) = ca[5]; SCHED();
    CV_MM(3, 2, 4); SCHED();
    CV_MM(3, 3, 3); SCHED();
    CV_MM(3, 4, 2); SCHED();
    CV_MM(3, 5, 1); SCHED();
    CV_MM(3, 6, 0); SCHED();
    CV_MM(3, 0, 7); SCHED();
    CV_MM(3, 1, 6); SCHED(); *(LAS f32x4*)(rb + 6 * 1024) = ca[6]; SCHED();
    CV_MM(3, 2, 5); SCHED();
    CV_MM(3, 3, 4); SCHED();
    CV_MM(3, 4, 3); SCHED();
    CV_MM(3, 5, 2); SCHED();
    CV_MM(3, 6, 1); SCHED();
    CV_MM(3, 7, 0); SCHED();
    *(LAS f32x4*)(rb + 7 * 1024) = ca[7];
  }
  v2u hfl;
  unsigned ph;
  {
    FRESH_IDS();
    const int c_lo = (7 * blk) >> 2, c_hi = (7 * blk + 6) >> 2;
    int cc = c_lo + (lane & 3);
    cc = cc > c_hi ? c_hi : cc;
    ph = (unsigned)(WS_FH + ((st >> 1) * 16 + cc) * FSTR);
    GLD2_SC1(hfl, ph, ws, 0);
  }
  STAMP(14);
  BAR();
  STAMP(15);
  {
    FRESH_IDS();
    const int ry = (lane & 3) * 256 + (((lane >> 2) ^ ((lane & 3) << 2)) * 16);
    const f32x4 c0 = *(LAS f32x4*)(L + L_RED + w * 1024 + ry) + *(LAS f32x4*)(L + L_RED + 8192 + w * 1024 + ry);
    const f32x4 c1 = *(LAS f32x4*)(L + L_RED + 16384 + w * 1024 + ry) + *(LAS f32x4*)(L + L_RED + 24576 + w * 1024 + ry);
    const unsigned so = (unsigned)(256 * w + 4 * lane);
    const v2u k0 = *(LAS v2u*)(L + L_U + 480 + so * 2), k1 = *(LAS v2u*)(L + L_U + 4608 + 480 + so * 2);
    const float D0 = blo(dd), D1 = bhi(dd);
    const v4u yv = v4u{pk_bf16(c0[0] + blo(k0.x) * D0, c1[0] + blo(k1.x) * D1), pk_bf16(c0[1] + bhi(k0.x) * D0, c1[1] + bhi(k1.x) * D1),
                       pk_bf16(c0[2] + blo(k0.y) * D0, c1[2] + blo(k1.y) * D1), pk_bf16(c0[3] + bhi(k0.y) * D0, c1[3] + bhi(k1.y) * D1)};
    const unsigned yo = so * 4;
    GST4_SC1(yo, yv, ws + WS_Y2 + (size_t)dpair * 8192, 0);
    WAITV(1);
    TIE(hfl);
  }
  STAMP(16);
#define COUNT_Y()                                                                   \
  do {                                                                              \
    if (COUNT_LAST(0, 7u) && (threadIdx.x & 63) == 0) {                             \
      const unsigned fo = (unsigned)(WS_FY + (n * 32 + st) * FSTR);                 \
      FLAG_ST(fo, tagv, ws);                                                        \
    }                                                                               \
  } while (0)
  const bool slow = !all_eq(hfl, tlo, thi);
  if (slow) {
    asm volatile("global_load_dwordx2 %0, %1, %2 sc1 ; SPIN\n\ts_waitcnt vmcnt(0) ; SPIN" : "=&v"(hfl) : "v"(ph), "s"(ws) : "memory");
    COUNT_Y();
    spin_flags(hfl, ph, ws, tlo, thi);
  }
  STAMP(17);
  v4u hf[HWIN];
  const char* const hb = ws + WS_H + (size_t)(4 * st * 56 + 7 * blk) * 1024 + 3072;
#define H_LOAD(x) GLD4_SC1(hf[(x) % HWIN], vl16, hb + ((((w >> 1) + 1 + (x) / 7) & 3) * 57344), ((x) % 7) * 1024 - 3072)
#pragma unroll
  for (int x = 0; x < HWIN; ++x) H_LOAD(x);
  STAMP(18);
  v4u w2f[28];
#pragma unroll
  for (int m_ = 0; m_ < 112; ++m_) TIE(w2r[m_]);
#pragma unroll
  for (int kl = 0; kl < 7; ++kl) {
#pragma unroll
    for (int hf_ = 0; hf_ < 2; ++hf_)
#pragma unroll
      for (int e2 = 0; e2 < 4; ++e2) {
        const unsigned lo_ = w2r[kl * 16 + 4 * e2 + hf_], hi_ = w2r[kl * 16 + 4 * e2 + 2 + hf_];
        w2f[kl * 4 + 2 * hf_][e2] = __builtin_amdgcn_perm(hi_, lo_, 0x05040100u);
        w2f[kl * 4 + 2 * hf_ + 1][e2] = __builtin_amdgcn_perm(hi_, lo_, 0x07060302u);
      }
#pragma unroll
    for (int j = 0; j < 4; ++j) TIE(w2f[kl * 4 + j]);
  }
  STAMP(28);
  unsigned pw, pr;
  {
    FRESH_IDS();
    pw = (unsigned)(w * 16384 + g * 256 + ((li ^ (g << 2)) * 16));
    const int r8 = lane >> 3, c8 = lane & 7, gg = c8 & 3;
    pr = (unsigned)((w >> 1) * 4096 + (c8 >> 2) * 2048 + gg * 256 + (((8 * (w & 1) + r8) ^ (gg << 2)) * 16));
  }
  v2u yf;
  unsigned py;
  v4u yt, qr;
  float gv;
  f32x4 a2[4], b0, b1;
#pragma unroll
  for (int x = 0; x < 28; ++x) {
    WAITV(g2_wait(x));
    TIE(hf[x % HWIN]);
    if (x == 0 && !slow) COUNT_Y();
    if (x == 0) STAMP(19);
    f32x4 r0, r1;
    if (x >= 21 && x < 27) {
      LAS char* const p_ = L + pr + ((((w & 6) + 2 + (x - 21)) & 7) * 16384);
      r0 = *(LAS f32x4*)(p_);
      r1 = *(LAS f32x4*)(p_ + 1024);
    }
    if (x % 7 == 0) {
#pragma unroll
      for (int j = 0; j < 4; ++j) a2[j] = f32x4{0.f, 0.f, 0.f, 0.f};
    }
    SCHED();
#pragma unroll
    for (int j = 0; j < 4; ++j) a2[j] = mfma16(w2f[(x % 7) * 4 + j], hf[x % HWIN], a2[j]);
    SCHED();
    if (x == 21) { b0 = r0; b1 = r1; }
    if (x > 21 && x < 27) { b0 += r0; b1 += r1; TIE(b0); TIE(b1); }
    if (x + HWIN < 28) H_LOAD(x + HWIN);
    if (x == XPOLL) {
      FRESH_IDS();
      py = lane < 32 ? (unsigned)(WS_FY + (n * 32 + 4 * w + (lane & 3)) * FSTR) : (unsigned)(WS_F0 + ((lane & 7) * 32 + st) * FSTR);
      GLD2_SC1(yf, py, ws, 0);
    }
    if (x == XCHK) {
      FRESH_IDS();
#pragma unroll
      for (int j = 0; j < 4; ++j) TIE(a2[j]);
      WAITV(g2_wait_poll());
      TIE(yf);
      STAMP(20);
      spin_flags(yf, py, ws, tlo, thi);
      STAMP(21);
      const unsigned yo = (unsigned)(g * 8192 + li * 16);
      GLD4_SC1(yt, yo, ws + WS_Y2 + (size_t)(32 * n + 4 * w) * 8192 + st * 256, 0);
      const unsigned go = (unsigned)((lane & 7) * 256 + (lane >> 3) * 4);
      GLD1_SC1(gv, go, ws + WS_GP + st * 2048 + w * 32, 0);
      const unsigned qo = (unsigned)((lane >> 3) * 1024 + (lane & 7) * 16);
      GLD4(qr, qo, q + (size_t)(64 * st + 8 * w) * 1024 + n * 128, 0);
    }
    if (x == 5) BAR();
    if (x % 7 == 6) {
      LAS char* const pb_ = L + pw + ((((w >> 1) + 1 + x / 7) & 3) * 4096);
#pragma unroll
      for (int j = 0; j < 4; ++j) *(LAS f32x4*)(pb_ + j * 1024) = a2[j];
    }
    if (x == 20) { BAR(); STAMP(22); }
    SCHED();
  }
  STAMP(23);
  float gt;
  {
    FRESH_IDS();
    WAITV(0);
    TIE(yt); TIE(gv); TIE(qr);
    *(LAS v4u*)(L + L_YT + (4 * w + g) * 256 + w * 16 + li * 16) = yt;
    gt = sigmoidf_(sum8(gv));
  }
  STAMP(24);
  BAR();
  STAMP(25);
  {
    FRESH_IDS();
    const int r8 = lane >> 3, c8 = lane & 7;
    LAS char* const p_ = L + pr + (w & 6) * 16384;
    b0 += *(LAS f32x4*)(p_) + *(LAS f32x4*)(p_ + 16384);
    b1 += *(LAS f32x4*)(p_ + 1024) + *(LAS f32x4*)(p_ + 16384 + 1024);
    unsigned yv[4];
#pragma unroll
    for (int i = 0; i < 4; ++i) yv[i] = *(LAS unsigned*)(L + L_YT + c8 * 1040 + i * 256 + (8 * w + r8) * 4);
    const float og = 1.0f - gt;
    v4u o;
#pragma unroll
    for (int i = 0; i < 4; ++i)
      o[i] = pk_bf16((gt * blo(qr[i])) * blo(yv[i]) + og * b0[i], (gt * bhi(qr[i])) * bhi(yv[i]) + og * b1[i]);
    const unsigned oo = (unsigned)(r8 * 1024 + c8 * 16);
    asm volatile(".Lpf_end%0:" ::"i"((g1 ? 2 : 0) + ROLE));
    GST4(oo, o, out + (size_t)(64 * st + 8 * w) * 1024 + n * 128, 0);
  }
}

extern "C" NOPK __global__ __launch_bounds__(512, 2) void fused_kernel(const char* __restrict__ q, const char* __restrict__ k, const char* __restrict__ v,
                                                       const char* __restrict__ sk, const char* __restrict__ ssmk, const char* __restrict__ ssmD,
                                                       const char* __restrict__ w1, const char* __restrict__ w2, const char* __restrict__ gw,
                                                       char* out, char* ws) {
#define TE
  unsigned cpf = 0;
  const int w = __builtin_amdgcn_readfirstlane(threadIdx.x >> 6);
  if (__builtin_expect((blockIdx.x >> 3) < 28, 1)) {
    if (w < 4) body<true, 0>(q, k, v, sk, ssmk, ssmD, w1, w2, gw, out, ws, w, cpf TE);
    else body<true, 1>(q, k, v, sk, ssmk, ssmD, w1, w2, gw, out, ws, w, cpf TE);
  } else {
    if (w < 4) body<false, 0>(q, k, v, sk, ssmk, ssmD, w1, w2, gw, out, ws, w, cpf TE);
    else body<false, 1>(q, k, v, sk, ssmk, ssmD, w1, w2, gw, out, ws, w, cpf TE);
  }
}

extern "C" void kernel_launch(void* const* d_in, const int* in_sizes, int n_in, void* d_out, int out_size, void* d_ws, size_t ws_size,
                              hipStream_t stream) {
  (void)in_sizes; (void)n_in; (void)out_size; (void)ws_size;
  (void)hipFuncSetAttribute((const void*)fused_kernel, hipFuncAttributeMaxDynamicSharedMemorySize, LDS_BYTES);
  fused_kernel<<<256, 512, LDS_BYTES, stream>>>((const char*)d_in[0], (const char*)d_in[1], (const char*)d_in[2], (const char*)d_in[3],
                                                (const char*)d_in[4], (const char*)d_in[5], (const char*)d_in[7], (const char*)d_in[8],
                                                (const char*)d_in[9], (char*)d_out, (char*)d_ws);
}
```

```cpp
#include <hip/hip_runtime.h>
#include <stdint.h>

typedef unsigned v4u __attribute__((ext_vector_type(4)));
typedef unsigned v2u __attribute__((ext_vector_type(2)));
typedef float f32x4 __attribute__((ext_vector_type(4)));
typedef float f32x2 __attribute__((ext_vector_type(2)));
typedef short bf16x8 __attribute__((ext_vector_type(8)));
typedef short s16x4 __attribute__((ext_vector_type(4)));
typedef __bf16 bf2 __attribute__((ext_vector_type(2)));
typedef unsigned long long u64;
template <int N> struct IC { static constexpr int value = N; };
#define LAS __attribute__((address_space(3)))
#if defined(__HIP_DEVICE_COMPILE__)
#define NOPK __attribute__((target("no-packed-fp32-ops")))
#else
#define NOPK
#endif

constexpr unsigned FSTR = 128;
constexpr unsigned WS_F0 = 786432;
constexpr unsigned WS_FH = 851968;
constexpr unsigned WS_FY = 917504;
constexpr unsigned WS_FG = 983040;
constexpr unsigned WS_GP = 16384;
constexpr unsigned WS_KVT = 1u << 20;
constexpr unsigned WS_Y2 = 3u << 20;
constexpr unsigned WS_H = 5u << 20;

constexpr unsigned L_KB = 131072;
constexpr unsigned L_VB = 140288;
constexpr unsigned L_YT = 131072;
constexpr unsigned L_CNT = 148480;
constexpr unsigned L_FILT = 0;
constexpr unsigned L_U = 33280;
constexpr unsigned L_RED = 65536;
constexpr unsigned LDS_BYTES = 148544;

constexpr int SPIN_CAP = 1 << 16;

NOPK __device__ __forceinline__ unsigned pk_bf16(float a, float b) {
  f32x2 v = {a, b};
  return __builtin_bit_cast(unsigned, __builtin_convertvector(v, bf2));
}
NOPK __device__ __forceinline__ float blo(unsigned u) { return __builtin_bit_cast(float, u << 16); }
NOPK __device__ __forceinline__ float bhi(unsigned u) { return __builtin_bit_cast(float, u & 0xffff0000u); }
NOPK __device__ __forceinline__ f32x4 mfma16(v4u a, v4u b, f32x4 c) {
  return __builtin_amdgcn_mfma_f32_16x16x32_bf16(__builtin_bit_cast(bf16x8, a), __builtin_bit_cast(bf16x8, b), c, 0, 0, 0);
}
NOPK __device__ __forceinline__ float sigmoidf_(float x) {
  return __builtin_amdgcn_rcpf(1.0f + __builtin_amdgcn_exp2f(-1.4426950408889634f * x));
}
NOPK __device__ __forceinline__ float silu1(float x) { return x * sigmoidf_(x); }
NOPK __device__ __forceinline__ float sum8(float x) {
  x += __builtin_bit_cast(float, __builtin_amdgcn_update_dpp(0, __builtin_bit_cast(int, x), 0xB1, 0xf, 0xf, true));
  x += __builtin_bit_cast(float, __builtin_amdgcn_update_dpp(0, __builtin_bit_cast(int, x), 0x4E, 0xf, 0xf, true));
  x += __builtin_bit_cast(float, __builtin_amdgcn_update_dpp(0, __builtin_bit_cast(int, x), 0x141, 0xf, 0xf, true));
  return x;
}

#define GLD1(dst, voff, sbase, imm) asm volatile("global_load_dword %0, %1, %2 offset:%3" : "=v"(dst) : "v"(voff), "s"(sbase), "i"(imm))
#define GLD1_SC1(dst, voff, sbase, imm) asm volatile("global_load_dword %0, %1, %2 offset:%3 sc1" : "=v"(dst) : "v"(voff), "s"(sbase), "i"(imm))
#define GLD2(dst, voff, sbase, imm) asm volatile("global_load_dwordx2 %0, %1, %2 offset:%3" : "=v"(dst) : "v"(voff), "s"(sbase), "i"(imm))
#define GLD2_SC1(dst, voff, sbase, imm) asm volatile("global_load_dwordx2 %0, %1, %2 offset:%3 sc1" : "=v"(dst) : "v"(voff), "s"(sbase), "i"(imm))
#define GLD4(dst, voff, sbase, imm) asm volatile("global_load_dwordx4 %0, %1, %2 offset:%3" : "=v"(dst) : "v"(voff), "s"(sbase), "i"(imm))
#define GLD4_SC1(dst, voff, sbase, imm) asm volatile("global_load_dwordx4 %0, %1, %2 offset:%3 sc1" : "=v"(dst) : "v"(voff), "s"(sbase), "i"(imm))
#define GST4_SC1(voff, data, sbase, imm) asm volatile("global_store_dwordx4 %0, %1, %2 offset:%3 sc1\n\ts_nop 1" ::"v"(voff), "v"(data), "s"(sbase), "i"(imm) : "memory")
#define GST4(voff, data, sbase, imm) asm volatile("global_store_dwordx4 %0, %1, %2 offset:%3 nt\n\ts_nop 1" ::"v"(voff), "v"(data), "s"(sbase), "i"(imm) : "memory")
#define FLAG_ST(voff, data, sbase) asm volatile("global_store_dwordx2 %0, %1, %2 sc1 ; FLAG" ::"v"(voff), "v"(data), "s"(sbase) : "memory")
#define GST1_SC1(voff, data, sbase, imm) asm volatile("global_store_dword %0, %1, %2 offset:%3 sc1" ::"v"(voff), "v"(data), "s"(sbase), "i"(imm) : "memory")
#define DMA16(ldsaddr, voff, sbase) asm volatile("s_mov_b32 m0, %0\n\ts_nop 0\n\tglobal_load_lds_dwordx4 %1, %2" ::"s"(ldsaddr), "v"(voff), "s"(sbase) : "memory")
#define WAITV(n) asm volatile("s_waitcnt vmcnt(%0)" ::"i"(n) : "memory")
#define BAR() asm volatile("s_waitcnt lgkmcnt(0)\n\ts_barrier" ::: "memory")
#define TIE(x) asm volatile("" : "+v"(x))
#define SCHED() __builtin_amdgcn_sched_barrier(0)
#define FRESH_IDS()                                         \
  int tid = threadIdx.x;                                    \
  asm volatile("" : "+v"(tid));                             \
  const int lane = tid & 63, li = lane & 15, g = lane >> 4; \
  (void)li; (void)g;

NOPK __device__ __forceinline__ bool all_eq(v2u f, unsigned tlo, unsigned thi) {
  return __builtin_amdgcn_ballot_w64(f.x == tlo && f.y == thi) == ~0ull;
}
NOPK __device__ __forceinline__ void spin_flags(v2u f, unsigned voff, const char* ws, unsigned tlo, unsigned thi) {
  int spins = 0;
  while (__builtin_expect(!all_eq(f, tlo, thi), 0)) {
    if (++spins > SPIN_CAP) break;
    __builtin_amdgcn_s_sleep(4);
    asm volatile("global_load_dwordx2 %0, %1, %2 sc1 ; SPIN\n\ts_waitcnt vmcnt(0) ; SPIN" : "=&v"(f) : "v"(voff), "s"(ws) : "memory");
  }
}

constexpr int HWIN = 8;
constexpr int XPOLL = 14;
constexpr int XCHK = 20;
constexpr int YCNT = 4;
constexpr int DPRE_A = 12;
constexpr int WT = 12, WT4 = 16, W2S = 4, W2D = 32;
#define YLATE (g1 ? 1 : 0)
#define g2_issued(x) (HWIN + YLATE + ((x) < 28 - HWIN ? (x) : 28 - HWIN) + ((x) > XPOLL ? 1 : 0) + ((x) > XCHK ? 3 : 0))
#define g2_wait(x) (g2_issued(x) - ((x) < HWIN ? (x) : g2_issued((x) - HWIN)) - 1)
#define g2_wait_poll() ((g2_issued(XCHK) + (XCHK + HWIN < 28 ? 1 : 0)) - (g2_issued(XPOLL) + (XPOLL + HWIN < 28 ? 1 : 0)) - 1)

template <bool g1, int ROLE>
NOPK __device__ __forceinline__ void body(const char* __restrict__ q, const char* __restrict__ k, const char* __restrict__ v,
                                     const char* __restrict__ sk, const char* __restrict__ ssmk, const char* __restrict__ ssmD,
                                     const char* __restrict__ w1, const char* __restrict__ w2, const char* __restrict__ gw,
                                     char* out, char* ws, const int w, const int nslow, unsigned cpf) {
  extern __shared__ __attribute__((aligned(16))) char smem_[];
  LAS char* const L = (LAS char*)smem_;
  const unsigned lbase = (unsigned)(uintptr_t)L;
  constexpr bool CW = ROLE == 0, HW = ROLE == 1;
  const int id = blockIdx.x, xcd = id & 7, loc = id >> 3;
  const int st = 4 * xcd + (loc >> 3), n = loc & 7;
  const int R = 4 * (xcd >> 1) + (loc >> 3), c = 7 * (xcd & 1) + (n > nslow ? n - 1 : n);
  const int dpair = 32 * n + st;
  const int j4 = w & 3;
  const int blk = (w + n + 2 * (loc >> 3)) & 7;

  if (HW) __builtin_amdgcn_s_setprio(3);
  constexpr bool G1C = g1 && CW;
  unsigned vx0 = 0, vx1 = 0, vw0 = 0, vw1 = 0;
  const char* const xb = q + (size_t)R * 131072 + j4 * 32768;
  const char* const wb = w1 + c * 256 + j4 * 57344;
#define DMA_ADDR()                                                                        \
  do {                                                                                    \
    FRESH_IDS();                                                                          \
    vx0 = (unsigned)((lane >> 3) * 1024 + (((lane & 7) ^ g) * 16));                       \
    vx1 = (unsigned)((8 + (lane >> 3)) * 1024 + (((lane & 7) ^ (4 + g)) * 16));           \
    vw0 = (unsigned)(g * 3584 + (((li >> 1) ^ g) * 32) + (li & 1) * 16);                  \
    vw1 = (unsigned)((8 + g) * 3584 + (((li >> 1) ^ (g | 4)) * 32) + (li & 1) * 16);      \
  } while (0)
  if (G1C) DMA_ADDR();
  const int rot = loc + xcd;
#define DMA_PIECE(ch, p)                                                                                      \
  do {                                                                                                        \
    if (g1 && ((ch) < 4 ? CW : HW)) {                                                                         \
      const unsigned sl_ = lbase + ((ch) & 3) * 32768 + j4 * 4096;                                            \
      const int kc_ = ((ch) + rot) & 7;                                                                       \
      if ((p) == 0) DMA16(sl_, vx0, xb + kc_ * 128);                                                          \
      if ((p) == 1) DMA16(sl_ + 16384, vw0, wb + kc_ * 229376);                                               \
      if ((p) == 2) DMA16(sl_ + 1024, vx1, xb + kc_ * 128);                                                   \
      if ((p) == 3) DMA16(sl_ + 16384 + 1024, vw0, wb + kc_ * 229376 + 14336);                                \
      if ((p) == 4) DMA16(sl_ + 2048, vx0, xb + kc_ * 128 + 16384);                                           \
      if ((p) == 5) DMA16(sl_ + 16384 + 2048, vw1, wb + kc_ * 229376);                                        \
      if ((p) == 6) DMA16(sl_ + 3072, vx1, xb + kc_ * 128 + 16384);                                           \
      if ((p) == 7) DMA16(sl_ + 16384 + 3072, vw1, wb + kc_ * 229376 + 14336);                                \
    }                                                                                                         \
  } while (0)
#define DMA_RANGE(lo, hi) _Pragma("unroll") for (int d_ = (lo); d_ < (hi); ++d_) DMA_PIECE(d_ >> 3, d_ & 7)
  DMA_RANGE(0, DPRE_A);

  u64 epoch;
  unsigned dd;
  asm volatile("s_load_dwordx2 %0, %1, 0x0" : "=s"(epoch) : "s"(ws));
  asm volatile("s_load_dword %0, %1, 0x0" : "=s"(dd) : "s"(ssmD + dpair * 4));

  unsigned vl16;
  { FRESH_IDS(); vl16 = (unsigned)(lane * 16); }
  v4u fhi[2], skr, gwr;
  v2u flo[2];
  if (HW) {
    FRESH_IDS();
    const unsigned ro = (unsigned)((lane >> 3) * 1024);
    const char* const kb_ = k + (size_t)st * 65536 + n * 128 + j4 * 16384;
    const char* const vb_ = v + (size_t)st * 65536 + n * 128 + j4 * 16384;
    const unsigned ka = ro + (((lane & 7) ^ ((2 * j4 + 1) & 7)) * 16), kc = ro + (((lane & 7) ^ ((2 * j4 + 2) & 7)) * 16);
    const unsigned va = ro + (((lane & 7) ^ (2 * j4)) * 16);
    const unsigned qo = ro + (lane & 7) * 16;
    DMA16(lbase + L_KB + 1024 + j4 * 2048, ka, kb_);
    DMA16(lbase + L_KB + 2048 + j4 * 2048, kc, kb_ + 8192);
    DMA16(lbase + L_VB + j4 * 2048, va, vb_);
    DMA16(lbase + L_VB + 1024 + j4 * 2048, ka, vb_ + 8192);
    if (j4 == 0) DMA16(lbase + L_KB, qo, kb_ - (st > 0 ? 8192 : 0));
    const unsigned so = (unsigned)((lane >> 3) * 16);
    GLD4(skr, so, sk + n * 512 + j4 * 128, 0);
    if (tid < 256 + 16) *(LAS unsigned*)(L + L_CNT + lane * 4) = 0u;
    WAITV(0);
  }
  unsigned tch[4];
  const int sj = st;
  if (!g1 && CW) {
    FRESH_IDS();
    __builtin_amdgcn_s_sleep(8);
    const int sw = sj * 4 + j4;
    const unsigned to = (unsigned)(lane * 128);
#pragma unroll
    for (int i = 0; i < 2; ++i) GLD1(tch[i], to, w2 + (size_t)sw * 14336 + i * 6144, 0);
#pragma unroll
    for (int i = 0; i < 2; ++i) GLD1(tch[2 + i], to, ssmk + (size_t)sw * 16384 + i * 8192, 0);
  }
  BAR();
  asm volatile("" : "+s"(epoch), "+s"(dd));
  const u64 tag = epoch * 6364136223846793005ull + 1442695040888963407ull;
  const unsigned tlo = (unsigned)tag, thi = (unsigned)(tag >> 32);
  const v2u tagv = v2u{tlo, thi};
  DMA_RANGE(DPRE_A, 24);
  if (G1C) WAITV(16);
#define KV_HALF(SEL, ta, tb, off)                                                                                          \
  do {                                                                                                                    \
    float kf[11], r_[8];                                                                                                  \
    const float t0 = blo(ta), t1 = bhi(ta), t2 = blo(tb), t3 = bhi(tb);                                                   \
    _Pragma("unroll") for (int i = 0; i < 11; ++i) kf[i] = SEL(kk[i]);                                                    \
    _Pragma("unroll") for (int i = 0; i < 8; ++i) r_[i] = (kf[i + 3] * t0 + kf[i + 2] * t1 + kf[i + 1] * t2 + kf[i] * t3) * SEL(vv[i]); \
    const v4u o_ = v4u{pk_bf16(r_[0], r_[1]), pk_bf16(r_[2], r_[3]), pk_bf16(r_[4], r_[5]), pk_bf16(r_[6], r_[7])};        \
    GST4_SC1(ko, o_, ws + WS_KVT + (size_t)n * 262144 + st * 128 + (off), 0);                                             \
  } while (0)
  if (HW) {
    FRESH_IDS();
    TIE(skr);
    const int sg = lane & 7, dp = 8 * j4 + (lane >> 3);
    const unsigned cb = (unsigned)((dp & 3) * 4), ck = (unsigned)(dp >> 2);
    unsigned kk[11], vv[8];
#pragma unroll
    for (int i = 0; i < 3; ++i) kk[i] = *(LAS unsigned*)(L + L_KB + (8 * sg + 5 + i) * 128 + ((ck ^ sg) * 16) + cb);
#pragma unroll
    for (int i = 0; i < 8; ++i) kk[3 + i] = *(LAS unsigned*)(L + L_KB + (8 * sg + 8 + i) * 128 + ((ck ^ ((sg + 1) & 7)) * 16) + cb);
#pragma unroll
    for (int i = 0; i < 8; ++i) vv[i] = *(LAS unsigned*)(L + L_VB + (8 * sg + i) * 128 + ((ck ^ sg) * 16) + cb);
    if (st == 0 && sg == 0) { kk[0] = 0u; kk[1] = 0u; kk[2] = 0u; }
    const unsigned ko = (unsigned)(dp * 8192 + sg * 16);
    KV_HALF(blo, skr.x, skr.y, 0);
    KV_HALF(bhi, skr.z, skr.w, 4096);
    SCHED();
  }
  BAR();
  if (HW) {
    if (g1) DMA_ADDR();
    __builtin_amdgcn_s_setprio(0);
    SCHED();
  }

  v4u xr[16];
#define GATE_LD()                                                                                                   \
  do {                                                                                                              \
    GLD4(gwr, vl16, gw, 0);                                                                                         \
    const char* const xb_ = q + (size_t)(sj * 64 + j4 * 16) * 1024 + 4096;                                          \
    _Pragma("unroll") for (int r = 0; r < 16; ++r) GLD4(xr[r], vl16, xb_ + (r >> 3) * 8192, ((r & 7) - 4) * 1024);  \
  } while (0)
  if (!g1 && CW) GATE_LD();
  f32x4 acc1[4][4];
#pragma unroll
  for (int a = 0; a < 4; ++a)
#pragma unroll
    for (int b = 0; b < 4; ++b) acc1[a][b] = f32x4{0.f, 0.f, 0.f, 0.f};
  const int wsx = w & 1, wfx = (w >> 1) & 1;
  unsigned ax0 = 0, aw0 = 0;
  if (G1C) {
    FRESH_IDS();
    __builtin_amdgcn_s_setprio(1);
    ax0 = (unsigned)((wsx * 64 + li) * 128 + ((g ^ (li >> 1)) * 16));
    const unsigned sg = (unsigned)((li >> 2) | ((g & 1) << 2));
    aw0 = (unsigned)(16384 + (8 * g + (li >> 2)) * 256 + (((4 * wfx) ^ sg) * 32) + (lane & 3) * 8);
  }
  v4u xf[2][4];
  v2u wfa[2][4], wfb[2][4];
#define RD_WA(hs, gq) wfa[(hs) & 1][gq] = __builtin_bit_cast(v2u, __builtin_amdgcn_ds_read_tr16_b64_v4i16((LAS s16x4*)(L + (((hs) >> 1) & 3) * 32768 + (aw0 ^ ((gq) * 32)) + ((hs) & 1) * 8192)))
#define RD_WB(hs, gq) wfb[(hs) & 1][gq] = __builtin_bit_cast(v2u, __builtin_amdgcn_ds_read_tr16_b64_v4i16((LAS s16x4*)(L + (((hs) >> 1) & 3) * 32768 + (aw0 ^ ((gq) * 32)) + ((hs) & 1) * 8192 + 1024)))
#define RD_X(hs, gq) xf[(hs) & 1][gq] = *(LAS v4u*)(L + (((hs) >> 1) & 3) * 32768 + (ax0 ^ (((hs) & 1) * 64)) + (gq) * 2048)
#define MM1(hs, t, gq) acc1[t][gq] = mfma16(v4u{wfa[(hs) & 1][t].x, wfa[(hs) & 1][t].y, wfb[(hs) & 1][t].x, wfb[(hs) & 1][t].y}, xf[(hs) & 1][gq], acc1[t][gq])
#define MM_GROUP(hs, gq) _Pragma("unroll") for (int t_ = 0; t_ < 4; ++t_) MM1(hs, t_, gq)

  unsigned w2r[112], vw2;
  { FRESH_IDS(); vw2 = (unsigned)(g * 4096 + li * 4); }
  const char* const w2b = w2 + (size_t)blk * 229376 + n * 128;
#define W2_LOAD(m) GLD1(w2r[m], vw2, w2b + ((m) >> 3) * 16384, (((m) >> 1) & 3) * 1024 + ((m) & 1) * 64)
#define W2_RANGE(lo, hi) _Pragma("unroll") for (int m_ = (lo); m_ < (hi); ++m_) { if (m_ >= 24 && (m_ & 3) == 0) WAITV(56); W2_LOAD(m_); }

#define HALF_STEP(hs, dlo, dhi)                                                                      \
  do {                                                                                               \
    if (G1C) {                                                                                       \
      _Pragma("unroll") for (int u_ = 0; u_ < 16; ++u_) {                                            \
        MM1(hs, u_ & 3, u_ >> 2);                                                                    \
        SCHED();                                                                                     \
        if (u_ == 0) RD_X((hs) + 1, 0);                                                              \
        else if (u_ < 9) { if (u_ & 1) RD_WA((hs) + 1, (u_ - 1) >> 1); else RD_WB((hs) + 1, (u_ - 1) >> 1); } \
        else if (u_ < 12) RD_X((hs) + 1, u_ - 8);                                                    \
        if ((u_ & 3) == 3) { DMA_RANGE((dlo) + (((dhi) - (dlo)) * (u_ >> 2)) / 4, (dlo) + (((dhi) - (dlo)) * ((u_ >> 2) + 1)) / 4); } \
        SCHED();                                                                                     \
      }                                                                                              \
    }                                                                                                \
  } while (0)
#define PIN_ACC1() do { if (G1C) { _Pragma("unroll") for (int a_ = 0; a_ < 16; ++a_) TIE(acc1[a_ & 3][a_ >> 2]); } } while (0)
  v2u pf;
  unsigned pf0;
  v4u kvt[2];
#define G1_HEAD(it, ncw, nhw)                                                                        \
  do {                                                                                               \
    PIN_ACC1();                                                                                      \
    if (G1C && (ncw) >= 0) WAITV(ncw);                                                               \
    if (HW && (nhw) >= 0 && (g1 || (it) == 2)) WAITV(g1 ? (nhw) : (nhw) - 16);                       \
    if (g1 || (it) == 2) BAR();                                                                      \
    if (HW && (it) == 2 && threadIdx.x == 256) {                                                     \
      const unsigned fo = (unsigned)(WS_F0 + (n * 32 + st) * FSTR);                                  \
      FLAG_ST(fo, tagv, ws);                                                                         \
    }                                                                                                \
    if (HW && (it) < 4) { DMA_RANGE(32 + 8 * (it), 40 + 8 * (it)); }                                 \
  } while (0)
  if (G1C) {
#pragma unroll
    for (int gq = 0; gq < 4; ++gq) { RD_WA(0, gq); RD_WB(0, gq); RD_X(0, gq); }
    SCHED();
  }
  HALF_STEP(0, 24, 32);
  G1_HEAD(0, 16, -1);
  if (HW) {
    FRESH_IDS();
    const int bi = tid & 255;
    const unsigned fo = (unsigned)((255 - bi) * 16);
    const unsigned fo2 = bi == 255 ? fo : fo - 8;
    const char* const fb_ = ssmk + (size_t)(2 * dpair) * 4096;
    GLD4(fhi[0], fo, fb_, 0);
    GLD2(flo[0], fo2, fb_, 0);
    GLD4(fhi[1], fo, fb_ + 4096, 0);
    GLD2(flo[1], fo2, fb_ + 4096, 0);
  }
  HALF_STEP(1, 0, 0); HALF_STEP(2, 0, 0);
  G1_HEAD(1, 8, -1);
  if (HW) { W2_RANGE(0, WT); }
  HALF_STEP(3, 0, 0); HALF_STEP(4, 0, 0);
  G1_HEAD(2, 0, 20 + WT);
  if (HW || g1) TIE(cpf);
  if (HW) { W2_RANGE(WT, 2 * WT); }
  HALF_STEP(5, 0, 0); HALF_STEP(6, 0, 0);
  G1_HEAD(3, -1, 20 + 2 * WT);
  if (HW) { W2_RANGE(2 * WT, 3 * WT); }
  HALF_STEP(7, 0, 0); HALF_STEP(8, 0, 0);
  G1_HEAD(4, -1, 16 + 3 * WT);
  if (HW) {
    FRESH_IDS();
    pf0 = (unsigned)(WS_F0 + (n * 32 + 8 * j4 + (lane & 7)) * FSTR);
    GLD2_SC1(pf, pf0, ws, 0);
    W2_RANGE(3 * WT, 3 * WT + WT4);
  }
  HALF_STEP(9, 0, 0); HALF_STEP(10, 0, 0);
  G1_HEAD(5, -1, 1 + WT + WT4);
  HALF_STEP(11, 0, 0); HALF_STEP(12, 0, 0);
  HALF_STEP(13, 0, 0); HALF_STEP(14, 0, 0);
#define COUNT_LAST(idx, last) \
  (__builtin_amdgcn_readfirstlane((threadIdx.x & 63) == 0 ? __hip_atomic_fetch_add((LAS unsigned*)(L + L_CNT + 4 * (idx)), 1u, __ATOMIC_RELAXED, __HIP_MEMORY_SCOPE_WORKGROUP) : 0u) == (last))
  if (CW) {
    char* const hb_ = ws + WS_H + (size_t)((8 * R + 4 * wsx) * 56 + 4 * c + 2 * wfx) * 1024;
#define SILU_TILE(i, kp)                                                                                               \
  do {                                                                                                                 \
    if (g1) {                                                                                                          \
    float s_[8];                                                                                                       \
    _Pragma("unroll") for (int t2 = 0; t2 < 2; ++t2)                                                                   \
      _Pragma("unroll") for (int r = 0; r < 4; ++r) s_[t2 * 4 + r] = silu1(acc1[2 * (kp) + t2][i][r]);                 \
    const v4u hv = v4u{pk_bf16(s_[0], s_[1]), pk_bf16(s_[2], s_[3]), pk_bf16(s_[4], s_[5]), pk_bf16(s_[6], s_[7])};    \
    GST4_SC1(vl16, hv, hb_ + (i) * 57344, (kp) * 1024);                                                                \
    }                                                                                                                  \
    SCHED();                                                                                                           \
  } while (0)
    if (!g1) {
      FRESH_IDS();
      WAITV(0);
      TIE(gwr); TIE(cpf);
#pragma unroll
      for (int i = 0; i < 4; ++i) TIE(tch[i]);
      float gsel = 0.f;
#pragma unroll
      for (int r = 0; r < 16; ++r) {
        TIE(xr[r]);
        float ga = 0.f;
#pragma unroll
        for (int i = 0; i < 4; ++i) ga += blo(xr[r][i]) * blo(gwr[i]) + bhi(xr[r][i]) * bhi(gwr[i]);
        ga = sum8(ga);
        ga += __builtin_bit_cast(float, __builtin_amdgcn_update_dpp(0, __builtin_bit_cast(int, ga), 0x128, 0xf, 0xf, true));
        gsel = li == r ? ga : gsel;
      }
      gsel += __builtin_bit_cast(float, __builtin_amdgcn_ds_bpermute((lane ^ 16) * 4, __builtin_bit_cast(int, gsel)));
      gsel += __builtin_bit_cast(float, __builtin_amdgcn_ds_bpermute((lane ^ 32) * 4, __builtin_bit_cast(int, gsel)));
      const float gs = sigmoidf_(gsel);
      const unsigned gpo = (unsigned)(li * 4);
      GST1_SC1(gpo, gs, ws + WS_GP + (sj * 64 + j4 * 16) * 4, 0);
      WAITV(0);
      if (COUNT_LAST(1, 3u) && (threadIdx.x & 63) == 0) {
        const unsigned fo = (unsigned)(WS_FG + sj * FSTR);
        FLAG_ST(fo, tagv, ws);
      }
    }
    if (g1) { MM_GROUP(15, 0); SCHED(); MM_GROUP(15, 1); SCHED(); }
    SILU_TILE(0, 0); W2_RANGE(0 * W2S, 1 * W2S); SILU_TILE(0, 1); W2_RANGE(1 * W2S, 2 * W2S);
    if (g1) { SCHED(); MM_GROUP(15, 2); SCHED(); }
    SILU_TILE(1, 0); W2_RANGE(2 * W2S, 3 * W2S); SILU_TILE(1, 1); W2_RANGE(3 * W2S, 4 * W2S);
    if (g1) { SCHED(); MM_GROUP(15, 3); SCHED(); }
    SILU_TILE(2, 0); W2_RANGE(4 * W2S, 5 * W2S); SILU_TILE(2, 1); W2_RANGE(5 * W2S, 6 * W2S);
    SCHED();
    SILU_TILE(3, 0); W2_RANGE(6 * W2S, 7 * W2S); SILU_TILE(3, 1);
    __builtin_amdgcn_s_setprio(0);
    if (!g1 && st == 0 && w == 0) {
      FRESH_IDS();
      const unsigned eo = (unsigned)(WS_F0 + lane * FSTR);
      for (int spins = 0; spins < SPIN_CAP; ++spins) {
        v2u f0, f1, f2, f3;
        asm volatile("global_load_dwordx2 %0, %4, %5 sc1 ; SPIN\n\tglobal_load_dwordx2 %1, %4, %6 sc1 ; SPIN\n\tglobal_load_dwordx2 %2, %4, %7 sc1 ; SPIN\n\t"
                     "global_load_dwordx2 %3, %4, %8 sc1 ; SPIN\n\ts_waitcnt vmcnt(0) ; SPIN"
                     : "=&v"(f0), "=&v"(f1), "=&v"(f2), "=&v"(f3)
                     : "v"(eo), "s"(ws), "s"(ws + 64 * FSTR), "s"(ws + 128 * FSTR), "s"(ws + 192 * FSTR) : "memory");
        if (all_eq(f0, tlo, thi) && all_eq(f1, tlo, thi) && all_eq(f2, tlo, thi) && all_eq(f3, tlo, thi)) break;
        __builtin_amdgcn_s_sleep(16);
      }
      if (lane == 0) { const unsigned z = 0; FLAG_ST(z, tagv, ws); }
    }
  } else {
    FRESH_IDS();
    WAITV(WT4);
    TIE(pf);
    spin_flags(pf, pf0, ws, tlo, thi);
    GLD4_SC1(kvt[0], vl16, ws + WS_KVT + (size_t)(2 * dpair) * 4096 + j4 * 1024, 0);
    GLD4_SC1(kvt[1], vl16, ws + WS_KVT + (size_t)(2 * dpair + 1) * 4096 + j4 * 1024, 0);
    W2_RANGE(3 * WT + WT4, 84);
    const int bi = tid & 255;
#pragma unroll
    for (int ch = 0; ch < 2; ++ch) {
      TIE(fhi[ch]); TIE(flo[ch]);
      if (bi == 255) flo[ch] = v2u{0u, 0u};
      const unsigned W2_ = flo[ch].x, W3 = flo[ch].y, W4 = fhi[ch].x, W5 = fhi[ch].y, W6 = fhi[ch].z, W7 = fhi[ch].w;
      const unsigned S3 = __builtin_amdgcn_alignbit(W3, W3, 16), S4 = __builtin_amdgcn_alignbit(W4, W4, 16), S5 = __builtin_amdgcn_alignbit(W5, W5, 16),
                     S6 = __builtin_amdgcn_alignbit(W6, W6, 16), S7 = __builtin_amdgcn_alignbit(W7, W7, 16);
      const unsigned T3 = (W3 & 0xffffu) | (W2_ & 0xffff0000u), T4 = (W4 & 0xffffu) | (W3 & 0xffff0000u), T5 = (W5 & 0xffffu) | (W4 & 0xffff0000u),
                     T6 = (W6 & 0xffffu) | (W5 & 0xffff0000u), T7 = (W7 & 0xffffu) | (W6 & 0xffff0000u);
      LAS char* fb = L + L_FILT + ch * 16640 + bi * 16;
      *(LAS v4u*)(fb) = v4u{S7, S6, S5, S4};
      *(LAS v4u*)(fb + 4160) = v4u{T7, T6, T5, T4};
      *(LAS v4u*)(fb + 8320) = v4u{S6, S5, S4, S3};
      *(LAS v4u*)(fb + 12480) = v4u{T6, T5, T4, T3};
    }
    if (j4 < 2) {
      const unsigned za = lane < 32 ? (unsigned)(L_U + j4 * 4608 + (lane < 30 ? lane : 29) * 16)
                                    : (unsigned)(L_FILT + j4 * 16640 + ((lane >> 1) & 3) * 4160 + 4096 + (lane & 1) * 16);
      *(LAS v4u*)(L + za) = v4u{0u, 0u, 0u, 0u};
    }
    W2_RANGE(84, 112);
    WAITV(112 - 3 * WT - WT4 < 56 ? 112 - 3 * WT - WT4 : 56);
    TIE(kvt[0]); TIE(kvt[1]);
    *(LAS v4u*)(L + L_U + 480 + j4 * 1024 + vl16) = kvt[0];
    *(LAS v4u*)(L + L_U + 4608 + 480 + j4 * 1024 + vl16) = kvt[1];
  }
  unsigned abo = 0, abo2 = 0, bbo = 0, rbo = 0;
  if (HW) {
    FRESH_IDS();
    const int ch = j4 >> 1, mh = j4 & 1;
    abo = (unsigned)(L_FILT + ch * 16640 + (3 - (li & 3)) * 4160 + 2 * (2028 + 8 * g - 4 * (li >> 2)) - 512 * 7 - 256 * mh - 192);
    abo2 = abo + 8;
    bbo = (unsigned)(L_U + ch * 4608 + 2 * (224 + 16 * li + 8 * g) - 256 * mh - 192);
    rbo = (unsigned)(L_RED + j4 * 8192 + g * 256 + ((li ^ (g << 2)) * 16));
    TIE(abo); TIE(abo2); TIE(bbo); TIE(rbo);
  }
  v2u hfl;
  unsigned ph;
#define FH_POLL()                                                                   \
  do {                                                                              \
    FRESH_IDS();                                                                    \
    const int c_lo = (7 * blk) >> 2, c_hi = (7 * blk + 6) >> 2;                     \
    int cc = c_lo + (lane & 3);                                                     \
    cc = cc > c_hi ? c_hi : cc;                                                     \
    ph = (unsigned)(WS_FH + ((st >> 1) * 16 + cc) * FSTR);                          \
    GLD2_SC1(hfl, ph, ws, 0);                                                       \
  } while (0)
  BAR();
  if (CW) {
    W2_RANGE(7 * W2S, 7 * W2S + W2D);
    if (g1) {
      WAITV(W2D);
      if (COUNT_LAST(1, 3u) && (threadIdx.x & 63) == 0) {
        const unsigned fo = (unsigned)(WS_FH + (R * 16 + c) * FSTR);
        FLAG_ST(fo, tagv, ws);
      }
    }
    W2_RANGE(7 * W2S + W2D, 112);
  } else {
    f32x4 ca[8];
#pragma unroll
    for (int i = 0; i < 8; ++i) ca[i] = f32x4{0.f, 0.f, 0.f, 0.f};
    LAS char* const bb = L + bbo;
#define CV_A0(mm, i) Aa[mm][i] = *(LAS v2u*)(L + abo + (3 - (mm)) * 64 + 512 * (7 - (i)))
#define CV_A1(mm, i) Ab[mm][i] = *(LAS v2u*)(L + abo2 + (3 - (mm)) * 64 + 512 * (7 - (i)))
#define CV_B(mm, j) B[mm][j] = *(LAS v4u*)(bb + (3 - (mm)) * 64 + 512 * (j))
#define CV_MM(mm, i, j) ca[(i) + (j)] = mfma16(v4u{Aa[mm][i].x, Aa[mm][i].y, Ab[mm][i].x, Ab[mm][i].y}, B[mm][j], ca[(i) + (j)])
    v2u Aa[4][8], Ab[4][8];
    v4u B[4][8];
    CV_A0(0, 0); SCHED(); CV_A1(0, 0); SCHED();
#pragma unroll
    for (int j = 0; j < 8; ++j) CV_B(0, j);
    SCHED();
    CV_A0(0, 1); SCHED(); CV_A1(0, 1); SCHED(); CV_A0(0, 2); SCHED(); CV_A1(0, 2); SCHED();
#define CV_STEP(mm) \
    CV_MM(mm, 0, 0); SCHED(); CV_A0(mm, 3); SCHED(); \
    CV_MM(mm, 0, 1); SCHED(); CV_A1(mm, 3); SCHED(); \
    CV_MM(mm, 0, 2); SCHED(); CV_B((mm) + 1, 0); SCHED(); \
    CV_MM(mm, 0, 3); SCHED(); \
    CV_MM(mm, 0, 4); SCHED(); CV_A0(mm, 4); SCHED(); \
    CV_MM(mm, 0, 5); SCHED(); CV_A1(mm, 4); SCHED(); \
    CV_MM(mm, 0, 6); SCHED(); CV_B((mm) + 1, 1); SCHED(); \
    CV_MM(mm, 0, 7); SCHED(); \
    CV_MM(mm, 1, 0); SCHED(); CV_A0(mm, 5); SCHED(); \
    CV_MM(mm, 1, 1); SCHED(); CV_A1(mm, 5); SCHED(); \
    CV_MM(mm, 1, 2); SCHED(); CV_B((mm) + 1, 2); SCHED(); \
    CV_MM(mm, 1, 3); SCHED(); \
    CV_MM(mm, 1, 4); SCHED(); CV_A0(mm, 6); SCHED(); \
    CV_MM(mm, 1, 5); SCHED(); CV_A1(mm, 6); SCHED(); \
    CV_MM(mm, 1, 6); SCHED(); CV_A0(mm, 7); SCHED(); \
    CV_MM(mm, 2, 0); SCHED(); \
    CV_MM(mm, 2, 1); SCHED(); CV_A1(mm, 7); SCHED(); \
    CV_MM(mm, 2, 2); SCHED(); CV_B((mm) + 1, 3); SCHED(); \
    CV_MM(mm, 2, 3); SCHED(); CV_B((mm) + 1, 4); SCHED(); \
    CV_MM(mm, 2, 4); SCHED(); \
    CV_MM(mm, 2, 5); SCHED(); CV_A0((mm) + 1, 0); SCHED(); \
    CV_MM(mm, 3, 0); SCHED(); CV_A1((mm) + 1, 0); SCHED(); \
    CV_MM(mm, 3, 1); SCHED(); CV_B((mm) + 1, 5); SCHED(); \
    CV_MM(mm, 3, 2); SCHED(); \
    CV_MM(mm, 3, 3); SCHED(); CV_B((mm) + 1, 6); SCHED(); \
    CV_MM(mm, 3, 4); SCHED(); CV_B((mm) + 1, 7); SCHED(); \
    CV_MM(mm, 4, 0); SCHED(); CV_A0((mm) + 1, 1); SCHED(); \
    CV_MM(mm, 4, 1); SCHED(); \
    CV_MM(mm, 4, 2); SCHED(); CV_A1((mm) + 1, 1); SCHED(); \
    CV_MM(mm, 4, 3); SCHED(); CV_A0((mm) + 1, 2); SCHED(); \
    CV_MM(mm, 5, 0); SCHED(); CV_A1((mm) + 1, 2); SCHED(); \
    CV_MM(mm, 5, 1); SCHED(); \
    CV_MM(mm, 5, 2); SCHED(); \
    CV_MM(mm, 6, 0); SCHED(); \
    CV_MM(mm, 6, 1); SCHED(); \
    CV_MM(mm, 7, 0); SCHED(); \
    do {} while (0)
    CV_STEP(0); CV_STEP(1); CV_STEP(2);
    LAS char* const rb = L + rbo;
    CV_MM(3, 0, 1); SCHED(); CV_A0(3, 3); SCHED();
    CV_MM(3, 0, 0); SCHED(); CV_A1(3, 3); SCHED();
    CV_MM(3, 1, 0); SCHED(); CV_A0(3, 4); SCHED();
    CV_MM(3, 0, 3); SCHED(); CV_A1(3, 4); SCHED();
    CV_MM(3, 0, 2); SCHED(); CV_A0(3, 5); SCHED();
    CV_MM(3, 1, 2); SCHED(); CV_A1(3, 5); SCHED();
    CV_MM(3, 1, 1); SCHED(); CV_A0(3, 6); SCHED(); *(LAS f32x4*)(rb + 0 * 1024) = ca[0]; SCHED();
    CV_MM(3, 2, 1); SCHED(); CV_A1(3, 6); SCHED();
    CV_MM(3, 2, 0); SCHED(); CV_A0(3, 7); SCHED(); *(LAS f32x4*)(rb + 1 * 1024) = ca[1]; SCHED();
    CV_MM(3, 3, 0); SCHED(); CV_A1(3, 7); SCHED();
    CV_MM(3, 0, 5); SCHED();
    CV_MM(3, 0, 4); SCHED();
    CV_MM(3, 1, 4); SCHED();
    CV_MM(3, 1, 3); SCHED(); *(LAS f32x4*)(rb + 2 * 1024) = ca[2]; SCHED();
    CV_MM(3, 2, 3); SCHED();
    CV_MM(3, 2, 2); SCHED(); *(LAS f32x4*)(rb + 3 * 1024) = ca[3]; SCHED();
    CV_MM(3, 3, 2); SCHED();
    CV_MM(3, 3, 1); SCHED();
    CV_MM(3, 4, 1); SCHED();
    CV_MM(3, 4, 0); SCHED();
    CV_MM(3, 5, 0); SCHED();
    FH_POLL(); SCHED();
    CV_MM(3, 0, 7); SCHED();
    CV_MM(3, 0, 6); SCHED();
    CV_MM(3, 1, 6); SCHED();
    CV_MM(3, 1, 5); SCHED(); *(LAS f32x4*)(rb + 4 * 1024) = ca[4]; SCHED();
    CV_MM(3, 2, 5); SCHED();
    CV_MM(3, 2, 4); SCHED(); *(LAS f32x4*)(rb + 5 * 1024) = ca[5]; SCHED();
    CV_MM(3, 3, 4); SCHED();
    CV_MM(3, 3, 3); SCHED();
    CV_MM(3, 4, 3); SCHED();
    CV_MM(3, 4, 2); SCHED();
    CV_MM(3, 5, 2); SCHED();
    CV_MM(3, 5, 1); SCHED();
    CV_MM(3, 6, 1); SCHED();
    CV_MM(3, 6, 0); SCHED();
    CV_MM(3, 7, 0); SCHED();
    *(LAS f32x4*)(rb + 6 * 1024) = ca[6];
    *(LAS f32x4*)(rb + 7 * 1024) = ca[7];
  }
  v4u w2f[28];
#define W2_PERM(klo, khi)                                                                                  \
  do {                                                                                                     \
    _Pragma("unroll") for (int m_ = 16 * (klo); m_ < 16 * (khi); ++m_) TIE(w2r[m_]);                       \
    _Pragma("unroll") for (int kl = (klo); kl < (khi); ++kl) {                                             \
      _Pragma("unroll") for (int hf_ = 0; hf_ < 2; ++hf_)                                                  \
        _Pragma("unroll") for (int e2 = 0; e2 < 4; ++e2) {                                                 \
          const unsigned lo_ = w2r[kl * 16 + 4 * e2 + hf_], hi_ = w2r[kl * 16 + 4 * e2 + 2 + hf_];         \
          w2f[kl * 4 + 2 * hf_][e2] = __builtin_amdgcn_perm(hi_, lo_, 0x05040100u);                        \
          w2f[kl * 4 + 2 * hf_ + 1][e2] = __builtin_amdgcn_perm(hi_, lo_, 0x07060302u);                    \
        }                                                                                                  \
      _Pragma("unroll") for (int j = 0; j < 4; ++j) TIE(w2f[kl * 4 + j]);                                  \
    }                                                                                                      \
  } while (0)
  constexpr int KPRE = CW ? 5 : 0;
  if (CW) FH_POLL();
  if (KPRE) { WAITV(113 - 16 * KPRE); W2_PERM(0, KPRE); }
  BAR();
  if constexpr (!YLATE) {
    FRESH_IDS();
    const int ry = (lane & 3) * 256 + (((lane >> 2) ^ ((lane & 3) << 2)) * 16);
    const f32x4 c0 = *(LAS f32x4*)(L + L_RED + w * 1024 + ry) + *(LAS f32x4*)(L + L_RED + 8192 + w * 1024 + ry);
    const f32x4 c1 = *(LAS f32x4*)(L + L_RED + 16384 + w * 1024 + ry) + *(LAS f32x4*)(L + L_RED + 24576 + w * 1024 + ry);
    const unsigned so = (unsigned)(256 * w + 4 * lane);
    const v2u k0 = *(LAS v2u*)(L + L_U + 480 + so * 2), k1 = *(LAS v2u*)(L + L_U + 4608 + 480 + so * 2);
    const float D0 = blo(dd), D1 = bhi(dd);
    const v4u yv = v4u{pk_bf16(c0[0] + blo(k0.x) * D0, c1[0] + blo(k1.x) * D1), pk_bf16(c0[1] + bhi(k0.x) * D0, c1[1] + bhi(k1.x) * D1),
                       pk_bf16(c0[2] + blo(k0.y) * D0, c1[2] + blo(k1.y) * D1), pk_bf16(c0[3] + bhi(k0.y) * D0, c1[3] + bhi(k1.y) * D1)};
    const unsigned yo = so * 4;
    GST4_SC1(yo, yv, ws + WS_Y2 + (size_t)dpair * 8192, 0);
  }
  WAITV(YLATE ? 0 : 1);
  TIE(hfl);
#define COUNT_Y()                                                                   \
  do {                                                                              \
    if (COUNT_LAST(0, 7u) && (threadIdx.x & 63) == 0) {                             \
      const unsigned fo = (unsigned)(WS_FY + (n * 32 + st) * FSTR);                 \
      FLAG_ST(fo, tagv, ws);                                                        \
    }                                                                               \
  } while (0)
  const bool slow = !all_eq(hfl, tlo, thi);
  if (slow) {
    asm volatile("global_load_dwordx2 %0, %1, %2 sc1 ; SPIN\n\ts_waitcnt vmcnt(0) ; SPIN" : "=&v"(hfl) : "v"(ph), "s"(ws) : "memory");
    if (!YLATE) COUNT_Y();
    spin_flags(hfl, ph, ws, tlo, thi);
  }
  v4u hf[HWIN];
  const char* const hb = ws + WS_H + (size_t)(4 * st * 56 + 7 * blk) * 1024 + 3072;
#define H_LOAD(x) GLD4_SC1(hf[(x) % HWIN], vl16, hb + ((((w >> 1) + 1 + (x) / 7) & 3) * 57344), ((x) % 7) * 1024 - 3072)
#pragma unroll
  for (int x = 0; x < HWIN; ++x) H_LOAD(x);
  if constexpr (YLATE) {
    FRESH_IDS();
    const int ry = (lane & 3) * 256 + (((lane >> 2) ^ ((lane & 3) << 2)) * 16);
    const f32x4 c0 = *(LAS f32x4*)(L + L_RED + w * 1024 + ry) + *(LAS f32x4*)(L + L_RED + 8192 + w * 1024 + ry);
    const f32x4 c1 = *(LAS f32x4*)(L + L_RED + 16384 + w * 1024 + ry) + *(LAS f32x4*)(L + L_RED + 24576 + w * 1024 + ry);
    const unsigned so = (unsigned)(256 * w + 4 * lane);
    const v2u k0 = *(LAS v2u*)(L + L_U + 480 + so * 2), k1 = *(LAS v2u*)(L + L_U + 4608 + 480 + so * 2);
    const float D0 = blo(dd), D1 = bhi(dd);
    const v4u yv = v4u{pk_bf16(c0[0] + blo(k0.x) * D0, c1[0] + blo(k1.x) * D1), pk_bf16(c0[1] + bhi(k0.x) * D0, c1[1] + bhi(k1.x) * D1),
                       pk_bf16(c0[2] + blo(k0.y) * D0, c1[2] + blo(k1.y) * D1), pk_bf16(c0[3] + bhi(k0.y) * D0, c1[3] + bhi(k1.y) * D1)};
    const unsigned yo = so * 4;
    GST4_SC1(yo, yv, ws + WS_Y2 + (size_t)dpair * 8192, 0);
  }
  W2_PERM(KPRE, 7);
#define PBASE(p) ((p) == 0 ? 98304 : (p) == 1 ? 0 : (p) == 2 ? 65536 : 32768)
  unsigned pw, pr;
  {
    FRESH_IDS();
    pw = (unsigned)(w * 4096 + g * 256 + ((li ^ (g << 2)) * 16));
    const int r8 = lane >> 3, c8 = lane & 7, gg = c8 & 3;
    pr = (unsigned)((c8 >> 2) * 2048 + gg * 256 + (((8 * (w & 1) + r8) ^ (gg << 2)) * 16));
  }
  v2u yf;
  unsigned py;
  v4u yt, qr;
  float gv;
  f32x4 a2[2][4], b0, b1;
#define PRE_RD(kk_, r0, r1)                                                                                     \
  do {                                                                                                          \
    LAS char* const p_ = L + pr + PBASE(2 - ((kk_) >> 1)) + ((((w & 6) + 2 + (kk_)) & 7) * 4096);               \
    r0 = *(LAS f32x4*)(p_);                                                                                     \
    r1 = *(LAS f32x4*)(p_ + 1024);                                                                              \
  } while (0)
#define P_WRITE(p, j) *(LAS f32x4*)(L + pw + PBASE(p) + (j) * 1024) = a2[(p) & 1][j]
  auto g2_step = [&](auto X) NOPK __attribute__((always_inline)) {
    constexpr int x = decltype(X)::value;
    constexpr int p = x / 7;
    WAITV(g2_wait(x));
    TIE(hf[x % HWIN]);
    if (!YLATE && x == 0 && !slow) COUNT_Y();
    if (YLATE && x == YCNT) { WAITV(YCNT); COUNT_Y(); }
    f32x4 r0, r1;
    constexpr int src = (x == 9 || x == 10) ? x - 5 : (x == 16 || x == 17) ? x - 14 : (x == 22 || x == 23) ? x - 22 : -1;
    if constexpr (src >= 0) PRE_RD(src, r0, r1);
    if (x % 7 == 0) {
#pragma unroll
      for (int j = 0; j < 4; ++j) a2[p & 1][j] = f32x4{0.f, 0.f, 0.f, 0.f};
    }
    SCHED();
#pragma unroll
    for (int j = 0; j < 4; ++j) {
      a2[p & 1][j] = mfma16(w2f[(x % 7) * 4 + j], hf[x % HWIN], a2[p & 1][j]);
      if constexpr (x % 7 == 0 && x > 0) { SCHED(); P_WRITE(p - 1, j); SCHED(); }
    }
    SCHED();
    if constexpr (x == 9) { b0 = r0; b1 = r1; }
    else if constexpr (src >= 0) { b0 += r0; b1 += r1; TIE(b0); TIE(b1); }
    if constexpr (x + HWIN < 28) H_LOAD(x + HWIN);
    if (x == XPOLL) {
      FRESH_IDS();
      py = lane < 32 ? (unsigned)(WS_FY + (n * 32 + 4 * w + (lane & 3)) * FSTR) : (unsigned)(WS_FG + st * FSTR);
      GLD2_SC1(yf, py, ws, 0);
    }
    if (x == XCHK) {
      FRESH_IDS();
#pragma unroll
      for (int j = 0; j < 4; ++j) TIE(a2[p & 1][j]);
      WAITV(g2_wait_poll());
      TIE(yf);
      spin_flags(yf, py, ws, tlo, thi);
      const unsigned yo = (unsigned)(g * 8192 + li * 16);
      GLD4_SC1(yt, yo, ws + WS_Y2 + (size_t)(32 * n + 4 * w) * 8192 + st * 256, 0);
      const unsigned go = (unsigned)((lane >> 3) * 4);
      GLD1_SC1(gv, go, ws + WS_GP + st * 256 + w * 32, 0);
      const unsigned qo = (unsigned)((lane >> 3) * 1024 + (lane & 7) * 16);
      GLD4(qr, qo, q + (size_t)(64 * st + 8 * w) * 1024 + n * 128, 0);
    }
    if (x == 8 || x == 15) BAR();
    if (x == 27) {
#pragma unroll
      for (int j = 0; j < 4; ++j) P_WRITE(p, j);
    }
    if (x == 21) BAR();
    SCHED();
  };
#define G2S(x) g2_step(IC<x>{})
  G2S(0); G2S(1); G2S(2); G2S(3); G2S(4); G2S(5); G2S(6); G2S(7); G2S(8); G2S(9); G2S(10); G2S(11); G2S(12); G2S(13);
  G2S(14); G2S(15); G2S(16); G2S(17); G2S(18); G2S(19); G2S(20); G2S(21); G2S(22); G2S(23); G2S(24); G2S(25); G2S(26); G2S(27);
  float gt, gq[8];
  {
    FRESH_IDS();
    WAITV(0);
    TIE(yt); TIE(gv); TIE(qr);
    *(LAS v4u*)(L + L_YT + (4 * w + g) * 256 + w * 16 + li * 16) = yt;
    gt = gv;
#pragma unroll
    for (int i = 0; i < 4; ++i) {
      gq[2 * i] = gt * blo(qr[i]);
      gq[2 * i + 1] = gt * bhi(qr[i]);
      TIE(gq[2 * i]); TIE(gq[2 * i + 1]);
    }
  }
  unsigned oo, ya;
  {
    FRESH_IDS();
    oo = (unsigned)((lane >> 3) * 1024 + (lane & 7) * 16);
    ya = (unsigned)(L_YT + (lane & 7) * 1040 + (8 * w + (lane >> 3)) * 4);
    pr += PBASE(3) + (w & 6) * 4096;
    TIE(oo); TIE(ya); TIE(pr);
  }
  BAR();
  {
    LAS char* const p_ = L + pr;
    b0 += *(LAS f32x4*)(p_) + *(LAS f32x4*)(p_ + 4096);
    b1 += *(LAS f32x4*)(p_ + 1024) + *(LAS f32x4*)(p_ + 4096 + 1024);
    unsigned yv[4];
#pragma unroll
    for (int i = 0; i < 4; ++i) yv[i] = *(LAS unsigned*)(L + ya + i * 256);
    const float og = 1.0f - gt;
    v4u o;
#pragma unroll
    for (int i = 0; i < 4; ++i) o[i] = pk_bf16(gq[2 * i] * blo(yv[i]) + og * b0[i], gq[2 * i + 1] * bhi(yv[i]) + og * b1[i]);
    asm volatile(".Lpf_end%0:" ::"i"((g1 ? 2 : 0) + ROLE));
    GST4(oo, o, out + (size_t)(64 * st + 8 * w) * 1024 + n * 128, 0);
  }
}

extern "C" NOPK __global__ __launch_bounds__(512, 2) void fused_kernel(const char* __restrict__ q, const char* __restrict__ k, const char* __restrict__ v,
                                                       const char* __restrict__ sk, const char* __restrict__ ssmk, const char* __restrict__ ssmD,
                                                       const char* __restrict__ w1, const char* __restrict__ w2, const char* __restrict__ gw,
                                                       char* out, char* ws) {
  unsigned cpf = 0;
  if (((blockIdx.x >> 3) & 7) == 3 && threadIdx.x < 256) {
    u64 pc;
    int l0, l1, l2, l3;
    asm volatile("s_getpc_b64 %0\n.Lpf_here:\n\ts_mov_b32 %1, .Lpf_end0-.Lpf_here\n\ts_mov_b32 %2, .Lpf_end1-.Lpf_here\n\t"
                 "s_mov_b32 %3, .Lpf_end2-.Lpf_here\n\ts_mov_b32 %4, .Lpf_end3-.Lpf_here"
                 : "=s"(pc), "=s"(l0), "=s"(l1), "=s"(l2), "=s"(l3));
    l0 = l0 > l1 ? l0 : l1;
    l2 = l2 > l3 ? l2 : l3;
    l0 = l0 > l2 ? l0 : l2;
    l0 = (l0 > 0 ? l0 : 0) & ~3;
    int off = (int)(((blockIdx.x >> 6) * 256 + threadIdx.x) * 64);
    off = off < l0 ? off : l0;
    asm volatile("global_load_dword %0, %1, %2" : "+v"(cpf) : "v"(off), "s"(pc));
  }
  const int w = __builtin_amdgcn_readfirstlane(threadIdx.x >> 6);
  const int nslow = (3 - (int)((uintptr_t)w2 >> 7)) & 7;
  if (__builtin_expect(((blockIdx.x >> 3) & 7) != nslow, 1)) {
    if (w < 4) body<true, 0>(q, k, v, sk, ssmk, ssmD, w1, w2, gw, out, ws, w, nslow, cpf);
    else body<true, 1>(q, k, v, sk, ssmk, ssmD, w1, w2, gw, out, ws, w, nslow, cpf);
  } else {
    if (w < 4) body<false, 0>(q, k, v, sk, ssmk, ssmD, w1, w2, gw, out, ws, w, nslow, cpf);
    else body<false, 1>(q, k, v, sk, ssmk, ssmD, w1, w2, gw, out, ws, w, nslow, cpf);
  }
}

extern "C" void kernel_launch(void* const* d_in, const int* in_sizes, int n_in, void* d_out, int out_size, void* d_ws, size_t ws_size,
                              hipStream_t stream) {
  (void)in_sizes; (void)n_in; (void)out_size; (void)ws_size;
  (void)hipFuncSetAttribute((const void*)fused_kernel, hipFuncAttributeMaxDynamicSharedMemorySize, LDS_BYTES);
  fused_kernel<<<256, 512, LDS_BYTES, stream>>>((const char*)d_in[0], (const char*)d_in[1], (const char*)d_in[2], (const char*)d_in[3],
                                                (const char*)d_in[4], (const char*)d_in[5], (const char*)d_in[7], (const char*)d_in[8],
                                                (const char*)d_in[9], (char*)d_out, (char*)d_ws);
}
```
